```python
import torch

def _softmax(x, axis=-1):
    return torch.softmax(x, dim=axis)

def _gelu(x):
    return torch.nn.functional.gelu(x, approximate='tanh')

def _silu(x):
    return torch.nn.functional.silu(x)

def _sigmoid(x):
    return torch.sigmoid(x)

def _layernorm(x, axis=-1, eps=1e-05):
    m = x.mean(dim=axis, keepdim=True)
    v = x.var(dim=axis, keepdim=True, unbiased=False)
    return (x - m) / torch.sqrt(v + eps)

def _scatter_add(tgt, idx, src, axis=0):
    return tgt.clone().index_add_(axis, idx, src)

def _segment_sum(src, seg, n):
    out = torch.zeros((n,) + src.shape[1:], dtype=src.dtype)
    return out.index_add_(0, seg, src)

def _pad(x, pw):
    flat = tuple((v for pair in reversed(pw) for v in pair))
    return torch.nn.functional.pad(x, flat)
kernel_name = 'adaLN_cond__ssm_conv_s4d__dense_activation_silu__prompt'

def setup_inputs():

    _w = lambda a: (lambda t: t.to(torch.bfloat16) if t.is_floating_point() else t)(torch.as_tensor(a))

    def _A_setup():
        Bsz, L, D, N = (1, 2048, 576, 128)
        return {'u': _w((lambda *s: torch.randn(*s, dtype=torch.bfloat16) * 0.05)(Bsz, L, D)), 'log_A_real': _w((lambda *s: torch.randn(*s, dtype=torch.bfloat16) * 3.2)(D, N) * 0.5), 'A_imag': _w((lambda *s: torch.randn(*s, dtype=torch.bfloat16) * 0.05)(D, N) * 3.14), 'C_real': _w((lambda *s: torch.randn(*s, dtype=torch.bfloat16) * 0.2)(D, N) / N ** 0.5), 'C_imag': _w((lambda *s: torch.randn(*s, dtype=torch.bfloat16) * 0.2)(D, N) / N ** 0.5), 'log_dt': _w((lambda *s: torch.randn(*s, dtype=torch.bfloat16) * 0.05)(D) * 0.5 - 4.0), 'D_skip': _w((lambda *s: torch.randn(*s, dtype=torch.bfloat16) * 0.1)(D))}
    _A_in = _A_setup()

    def _B_setup():
        B, S, D, F = (1, 2048, 576, 4608)
        return {'x': _w((lambda *s: torch.randn(*s, dtype=torch.bfloat16) * 0.05)(B, S, D)), 'w1': _w((lambda *s: torch.randn(*s, dtype=torch.bfloat16) * 3.2)(D, F) * D ** (-0.5)), 'w2': _w((lambda *s: torch.randn(*s, dtype=torch.bfloat16) * 0.4)(F, D) * F ** (-0.5))}
    _B_in = _B_setup()
    return {'A_u': _A_in['u'], 'A_log_A_real': _A_in['log_A_real'], 'A_A_imag': _A_in['A_imag'], 'A_C_real': _A_in['C_real'], 'A_C_imag': _A_in['C_imag'], 'A_log_dt': _A_in['log_dt'], 'A_D_skip': _A_in['D_skip'], 'B_x': _B_in['x'], 'B_w1': _B_in['w1'], 'B_w2': _B_in['w2'], 'c': _w((lambda *s: torch.randn(*s, dtype=torch.bfloat16) * 0.2)(1, 576) * 8.0), 'w_mod': _w((lambda *s: torch.randn(*s, dtype=torch.bfloat16) * 0.2)(576, 6 * 576) * 576 ** (-0.5) * 8.0)}

def reference(A_u, A_log_A_real, A_A_imag, A_C_real, A_C_imag, A_log_dt, A_D_skip, B_x, B_w1, B_w2, c, w_mod):

    def _A_ref(u, log_A_real, A_imag, C_real, C_imag, log_dt, D_skip):
        L = u.shape[1]
        dt = torch.exp(log_dt)
        A = -torch.exp(log_A_real) + 1j * A_imag
        C = (C_real + 1j * C_imag) * dt[:, None]
        pos = (lambda x, dt: x.to(dt))(torch.arange(L), log_dt.dtype)
        P = torch.exp((A * dt[:, None])[:, :, None] * pos[None, None, :])
        K = 2.0 * torch.einsum('dn,dnl->dl', C, P).real
        L2 = 2 * L
        u_t = (lambda x, *d: x.permute(*d))(u, 0, 2, 1)
        Kf = (lambda x, *a, **k: torch.fft.rfft(x.float(), *a, **k))(K, L2)
        Uf = (lambda x, *a, **k: torch.fft.rfft(x.float(), *a, **k))(u_t, L2)
        y = (lambda x, *a, **k: torch.fft.irfft(x, *a, **k).to(torch.bfloat16))(Uf * Kf[None, :, :], L2)[:, :, :L]
        y = (lambda x, *d: x.permute(*d))(y, 0, 2, 1)
        return y + u * D_skip[None, None, :]

    def _B_ref(x, w1, w2):
        h = torch.einsum('bsd,df->bsf', x, w1)
        h = _silu(h)
        out = torch.einsum('bsf,fd->bsd', h, w2)
        return out
    x = A_u
    mod = (_silu(c) @ w_mod).reshape(c.shape[0], 6, 1, -1)
    sa, ca, ga, sb, cb, gb = (mod[:, 0].clone(), mod[:, 1].clone(), mod[:, 2].clone(), mod[:, 3].clone(), mod[:, 4].clone(), mod[:, 5].clone())
    h = _layernorm(x, axis=-1) * (1 + ca) + sa
    x = x + ga * _A_ref(h, A_log_A_real, A_A_imag, A_C_real, A_C_imag, A_log_dt, A_D_skip)
    h = _layernorm(x, axis=-1) * (1 + cb) + sb
    x = x + gb * _B_ref(h, B_w1, B_w2)
    return x
```

```cpp
#include <hip/hip_runtime.h>
#include <hip/hip_bf16.h>
#include <stdint.h>

typedef unsigned short u16;
#define L_ 2048
#define D_ 576
#define N_ 128
#define F_ 4608

#define POS_BF16 0

__device__ __forceinline__ float bf2f(u16 v) { return __uint_as_float(((unsigned)v) << 16); }
__device__ __forceinline__ u16 f2bf(float f) {
  unsigned u = __float_as_uint(f);
  u += 0x7fffu + ((u >> 16) & 1u);
  return (u16)(u >> 16);
}
__device__ __forceinline__ float rbf(float f) { return bf2f(f2bf(f)); }

__global__ void k_mod(const u16* c, const u16* wmod, float* mod) {
  int j = blockIdx.x * blockDim.x + threadIdx.x;
  if (j >= 6 * D_) return;
  float acc = 0.f;
  for (int k = 0; k < D_; ++k) {
    float cv = bf2f(c[k]);
    float s = rbf(cv / (1.f + __expf(-cv)));
    acc += s * bf2f(wmod[k * 6 * D_ + j]);
  }
  mod[j] = rbf(acc);
}

__global__ void k_kgen(const u16* lar, const u16* aim, const u16* cre, const u16* cim, const u16* ldt, float* K) {
  int d = blockIdx.y;
  int l = blockIdx.x * blockDim.x + threadIdx.x;
  float dt = rbf(expf(bf2f(ldt[d])));
  float pos = POS_BF16 ? rbf((float)l) : (float)l;
  float acc = 0.f;
  for (int n = 0; n < N_; ++n) {
    float ar = -rbf(expf(bf2f(lar[d * N_ + n])));
    float ai = bf2f(aim[d * N_ + n]);
    float cr = bf2f(cre[d * N_ + n]) * dt, ci = bf2f(cim[d * N_ + n]) * dt;
    float zr = ar * dt * pos, zi = ai * dt * pos;
    float e = expf(zr);
    float s, co;
    sincosf(zi, &s, &co);
    acc += cr * (e * co) - ci * (e * s);
  }
  K[d * L_ + l] = 2.f * acc;
}

__global__ void k_ln(const u16* x, const float* shift, const float* scale, u16* h) {
  int t = blockIdx.x;
  int lane = threadIdx.x;
  float v[9];
  float s = 0.f;
  for (int i = 0; i < 9; ++i) { v[i] = bf2f(x[t * D_ + lane + 64 * i]); s += v[i]; }
  for (int o = 32; o; o >>= 1) s += __shfl_xor(s, o);
  float m = s / D_;
  float q = 0.f;
  for (int i = 0; i < 9; ++i) { float dd = v[i] - m; q += dd * dd; }
  for (int o = 32; o; o >>= 1) q += __shfl_xor(q, o);
  float r = rsqrtf(q / D_ + 1e-5f);
  for (int i = 0; i < 9; ++i) {
    int d = lane + 64 * i;
    h[t * D_ + d] = f2bf((v[i] - m) * r * (1.f + scale[d]) + shift[d]);
  }
}

__global__ void k_conv(const u16* x, const u16* h, const float* K, const u16* dskip, const float* ga, u16* x1) {
  int d = blockIdx.x * blockDim.x + threadIdx.x;
  int t = blockIdx.y;
  if (d >= D_) return;
  float acc = 0.f;
  for (int l = 0; l <= t; ++l) acc += K[d * L_ + l] * bf2f(h[(t - l) * D_ + d]);
  float y = rbf(acc) + rbf(bf2f(h[t * D_ + d]) * bf2f(dskip[d]));
  x1[t * D_ + d] = f2bf(bf2f(x[t * D_ + d]) + ga[d] * y);
}

template <int MODE>
__global__ void k_gemm(const u16* A, const u16* B, int Kd, int Nd, const u16* res, const float* g, u16* out) {
  __shared__ float sa[16][17], sb[16][17];
  int tx = threadIdx.x, ty = threadIdx.y;
  int row = blockIdx.y * 16 + ty, col = blockIdx.x * 16 + tx;
  float acc = 0.f;
  for (int k0 = 0; k0 < Kd; k0 += 16) {
    sa[ty][tx] = bf2f(A[row * Kd + k0 + tx]);
    sb[ty][tx] = bf2f(B[(k0 + ty) * Nd + col]);
    __syncthreads();
    for (int k = 0; k < 16; ++k) acc += sa[ty][k] * sb[k][tx];
    __syncthreads();
  }
  if (MODE == 0) {
    float v = rbf(acc);
    out[row * Nd + col] = f2bf(v / (1.f + __expf(-v)));
  } else {
    out[row * Nd + col] = f2bf(bf2f(res[row * Nd + col]) + g[col] * rbf(acc));
  }
}

extern "C" void kernel_launch(void* const* d_in, const int* in_sizes, int n_in, void* d_out, int out_size, void* d_ws,
                              size_t ws_size, hipStream_t stream) {
  const u16* u = (const u16*)d_in[0];
  const u16* lar = (const u16*)d_in[1];
  const u16* aim = (const u16*)d_in[2];
  const u16* cre = (const u16*)d_in[3];
  const u16* cim = (const u16*)d_in[4];
  const u16* ldt = (const u16*)d_in[5];
  const u16* dsk = (const u16*)d_in[6];
  const u16* w1 = (const u16*)d_in[8];
  const u16* w2 = (const u16*)d_in[9];
  const u16* c = (const u16*)d_in[10];
  const u16* wmod = (const u16*)d_in[11];
  char* ws = (char*)d_ws;
  float* mod = (float*)ws;
  float* K = (float*)(ws + (1 << 16));
  u16* h = (u16*)(ws + (8 << 20));
  u16* x1 = (u16*)(ws + (12 << 20));
  u16* h2 = (u16*)(ws + (16 << 20));
  u16* H = (u16*)(ws + (20 << 20));
  k_mod<<<(6 * D_ + 63) / 64, 64, 0, stream>>>(c, wmod, mod);
  k_kgen<<<dim3(L_ / 256, D_), 256, 0, stream>>>(lar, aim, cre, cim, ldt, K);
  k_ln<<<L_, 64, 0, stream>>>(u, mod + 0 * D_, mod + 1 * D_, h);
  k_conv<<<dim3(D_ / 64, L_), 64, 0, stream>>>(u, h, K, dsk, mod + 2 * D_, x1);
  k_ln<<<L_, 64, 0, stream>>>(x1, mod + 3 * D_, mod + 4 * D_, h2);
  k_gemm<0><<<dim3(F_ / 16, L_ / 16), dim3(16, 16), 0, stream>>>(h2, w1, D_, F_, nullptr, nullptr, H);
  k_gemm<1><<<dim3(D_ / 16, L_ / 16), dim3(16, 16), 0, stream>>>(H, w2, F_, D_, x1, mod + 5 * D_, (u16*)d_out);
}
```

```cpp
#include <hip/hip_runtime.h>
#include <stdint.h>

typedef unsigned short u16;
typedef unsigned int u32;
typedef __attribute__((ext_vector_type(4))) unsigned u32x4;
typedef __attribute__((ext_vector_type(2))) unsigned u32x2;
typedef __attribute__((ext_vector_type(4))) float f32x4;
typedef __attribute__((ext_vector_type(2))) float f32x2;
typedef __attribute__((ext_vector_type(8))) __bf16 bf16x8;
typedef __attribute__((ext_vector_type(2))) __bf16 bf16x2;
typedef __attribute__((ext_vector_type(8))) _Float16 f16x8;
typedef __attribute__((ext_vector_type(2))) _Float16 f16x2;
typedef __attribute__((ext_vector_type(4))) short s16x4;
typedef __attribute__((address_space(3))) s16x4* lds_s16x4_ptr;
typedef __attribute__((address_space(1))) unsigned gu32;

#define DI __device__ __forceinline__

constexpr int L_ = 2048, D_ = 576, N_ = 128, F_ = 4608;
constexpr int MODN = 6 * D_;
constexpr int KRP = 2064;
constexpr float LOG2E = 1.44269504088896341f;
constexpr float LN2 = 0.69314718055994531f;

constexpr u32 WS_MODP = 0;
constexpr u32 WS_STATS = 65536;
constexpr u32 WS_VP = 131072;
constexpr u32 WS_FLAG = 262144;
constexpr u32 WS_GB = 327680;
constexpr u32 WS_UT = 1u << 19;
constexpr u32 WS_KR = 3u << 20;
constexpr u32 WS_X1S = 8u << 20;
constexpr u32 WS_WST = 21u << 19;
constexpr u32 WS_HF = 21u << 20;
constexpr u32 WS_PART = 39u << 20;
constexpr u32 WS_END = 47u << 20;

DI float bf_lo(u32 v) { return __uint_as_float(v << 16); }
DI float bf_hi(u32 v) { return __uint_as_float(v & 0xffff0000u); }
DI float bf2f(u16 v) { return __uint_as_float(((u32)v) << 16); }
DI u32 pack2bf(float lo, float hi) {
  f32x2 f = {lo, hi};
  return __builtin_bit_cast(u32, __builtin_convertvector(f, bf16x2));
}
DI float rbf(float x) { return __uint_as_float(pack2bf(x, x) << 16); }
DI u32 pack2h(float lo, float hi) {
  f32x2 f = {lo, hi};
  return __builtin_bit_cast(u32, __builtin_convertvector(f, f16x2));
}
DI u32x2 tr_read(const char* p) {
  s16x4 v = __builtin_amdgcn_ds_read_tr16_b64_v4i16((lds_s16x4_ptr)(p));
  return __builtin_bit_cast(u32x2, v);
}
DI f32x4 mfma_bf16(u32x4 a, u32x4 b, f32x4 c) {
  return __builtin_amdgcn_mfma_f32_16x16x32_bf16(__builtin_bit_cast(bf16x8, a), __builtin_bit_cast(bf16x8, b), c, 0, 0, 0);
}
DI f32x4 mfma_f16(u32x4 a, u32x4 b, f32x4 c) {
  return __builtin_amdgcn_mfma_f32_16x16x32_f16(__builtin_bit_cast(f16x8, a), __builtin_bit_cast(f16x8, b), c, 0, 0, 0);
}
DI void glds16(const void* g, void* l) {
  __builtin_amdgcn_global_load_lds((const unsigned*)g, (unsigned*)l, 16, 0, 0);
}
DI void swap32(u32& a, u32& b) { asm volatile("s_nop 7\n\tv_permlane32_swap_b32 %0, %1\n\ts_nop 1" : "+v"(a), "+v"(b)); }
DI void swap16(u32& a, u32& b) { asm volatile("s_nop 7\n\tv_permlane16_swap_b32 %0, %1\n\ts_nop 1" : "+v"(a), "+v"(b)); }
DI void swap32x4(u32& a0, u32& b0, u32& a1, u32& b1, u32& a2, u32& b2, u32& a3, u32& b3) {
  asm volatile("s_nop 7\n\tv_permlane32_swap_b32 %0, %1\n\tv_permlane32_swap_b32 %2, %3\n\tv_permlane32_swap_b32 %4, %5\n\tv_permlane32_swap_b32 %6, %7\n\ts_nop 1"
               : "+v"(a0), "+v"(b0), "+v"(a1), "+v"(b1), "+v"(a2), "+v"(b2), "+v"(a3), "+v"(b3));
}
DI void swap16x2(u32& a0, u32& b0, u32& a1, u32& b1) {
  asm volatile("s_nop 7\n\tv_permlane16_swap_b32 %0, %1\n\tv_permlane16_swap_b32 %2, %3\n\ts_nop 1" : "+v"(a0), "+v"(b0), "+v"(a1), "+v"(b1));
}
#define BAR() __builtin_amdgcn_s_barrier()
#define SCHED() __builtin_amdgcn_sched_barrier(0)
#define WAITV(n) asm volatile("s_waitcnt vmcnt(" #n ")" ::: "memory")
#define WAITL0() asm volatile("s_waitcnt lgkmcnt(0)" ::: "memory")
#define MKRSRC(p, bytes) __builtin_amdgcn_make_buffer_rsrc((void*)(p), 0, (int)(bytes), 0x00020000)
#define ST16(rs, off, v) __builtin_amdgcn_raw_buffer_store_b128((v), (rs), (int)(off), 0, 16)
#define ST16S(rs, voff, soff, v) __builtin_amdgcn_raw_buffer_store_b128((v), (rs), (int)(voff), (int)(soff), 16)
#define ST8(rs, off, v) __builtin_amdgcn_raw_buffer_store_b64((v), (rs), (int)(off), 0, 16)
#define ST4(rs, off, v) __builtin_amdgcn_raw_buffer_store_b32((v), (rs), (int)(off), 0, 16)


DI float add4(float a, float b, float c, float d) {
  float x = a + b; asm volatile("" : "+v"(x));
  float y = c + d; asm volatile("" : "+v"(y));
  return x + y;
}
DI float smul(float a, float b) { float d; asm("v_mul_f32 %0, %1, %2" : "=v"(d) : "v"(a), "v"(b)); return d; }
DI float sadd(float a, float b) { float d; asm("v_add_f32 %0, %1, %2" : "=v"(d) : "v"(a), "v"(b)); return d; }
DI float ssub(float a, float b) { float d; asm("v_sub_f32 %0, %1, %2" : "=v"(d) : "v"(a), "v"(b)); return d; }
DI float sfma(float a, float b, float c) { float d; asm("v_fma_f32 %0, %1, %2, %3" : "=v"(d) : "v"(a), "v"(b), "v"(c)); return d; }
DI float modval(const float* modp, int idx) { return rbf(modp[idx] + modp[MODN + idx]); }

#define CODE_PF(sym, off, var) do { unsigned long long pc_; int dl_; \
    asm volatile("s_getpc_b64 %0\n.Lpf%=:\n\ts_mov_b32 %1, " #sym "-.Lpf%=" : "=s"(pc_), "=s"(dl_)); \
    var = *(const u32*)(pc_ + (long long)dl_ + (off)); } while (0)

__device__ const __attribute__((aligned(16))) u16 KIDX[2048] = {
  2560,2560,2560,2560,2560,2556,2556,2556,2556,2556,2556,2556,2552,2552,2552,2552,2552,2552,2552,2552,2552,2548,2548,2548,2548,2548,2548,2548,2544,2544,2544,2544,
  2544,2544,2544,2544,2544,2540,2540,2540,2540,2540,2540,2540,2536,2536,2536,2536,2536,2536,2536,2536,2536,2532,2532,2532,2532,2532,2532,2532,2528,2528,2528,2528,
  2528,2528,2528,2528,2528,2524,2524,2524,2524,2524,2524,2524,2520,2520,2520,2520,2520,2520,2520,2520,2520,2516,2516,2516,2516,2516,2516,2516,2512,2512,2512,2512,
  2512,2512,2512,2512,2512,2508,2508,2508,2508,2508,2508,2508,2504,2504,2504,2504,2504,2504,2504,2504,2504,2500,2500,2500,2500,2500,2500,2500,2496,2496,2496,2496,
  2496,2496,2496,2496,2496,2492,2492,2492,2492,2492,2492,2492,2488,2488,2488,2488,2488,2488,2488,2488,2488,2484,2484,2484,2484,2484,2484,2484,2480,2480,2480,2480,
  2480,2480,2480,2480,2480,2476,2476,2476,2476,2476,2476,2476,2472,2472,2472,2472,2472,2472,2472,2472,2472,2468,2468,2468,2468,2468,2468,2468,2464,2464,2464,2464,
  2464,2464,2464,2464,2464,2460,2460,2460,2460,2460,2460,2460,2456,2456,2456,2456,2456,2456,2456,2456,2456,2452,2452,2452,2452,2452,2452,2452,2448,2448,2448,2448,
  2448,2448,2448,2448,2448,2444,2444,2444,2444,2444,2444,2444,2440,2440,2440,2440,2440,2440,2440,2440,2440,2436,2436,2436,2436,2436,2436,2436,2432,2432,2432,2432,
  2432,2432,2432,2432,2432,2428,2428,2428,2428,2428,2428,2428,2424,2424,2424,2424,2424,2424,2424,2424,2424,2420,2420,2420,2420,2420,2420,2420,2416,2416,2416,2416,
  2416,2416,2416,2416,2416,2412,2412,2412,2412,2412,2412,2412,2408,2408,2408,2408,2408,2408,2408,2408,2408,2404,2404,2404,2404,2404,2404,2404,2400,2400,2400,2400,
  2400,2400,2400,2400,2400,2396,2396,2396,2396,2396,2396,2396,2392,2392,2392,2392,2392,2392,2392,2392,2392,2388,2388,2388,2388,2388,2388,2388,2384,2384,2384,2384,
  2384,2384,2384,2384,2384,2380,2380,2380,2380,2380,2380,2380,2376,2376,2376,2376,2376,2376,2376,2376,2376,2372,2372,2372,2372,2372,2372,2372,2368,2368,2368,2368,
  2368,2368,2368,2368,2368,2364,2364,2364,2364,2364,2364,2364,2360,2360,2360,2360,2360,2360,2360,2360,2360,2356,2356,2356,2356,2356,2356,2356,2352,2352,2352,2352,
  2352,2352,2352,2352,2352,2348,2348,2348,2348,2348,2348,2348,2344,2344,2344,2344,2344,2344,2344,2344,2344,2340,2340,2340,2340,2340,2340,2340,2336,2336,2336,2336,
  2336,2336,2336,2336,2336,2332,2332,2332,2332,2332,2332,2332,2328,2328,2328,2328,2328,2328,2328,2328,2328,2324,2324,2324,2324,2324,2324,2324,2320,2320,2320,2320,
  2320,2320,2320,2320,2320,2316,2316,2316,2316,2316,2316,2316,2312,2312,2312,2312,2312,2312,2312,2312,2312,2308,2308,2308,2308,2308,2308,2308,2304,2304,2304,2304,
  2304,2304,2304,2304,2304,2300,2300,2300,2300,2300,2300,2300,2296,2296,2296,2296,2296,2296,2296,2296,2296,2292,2292,2292,2292,2292,2292,2292,2288,2288,2288,2288,
  2288,2288,2288,2288,2288,2284,2284,2284,2284,2284,2284,2284,2280,2280,2280,2280,2280,2280,2280,2280,2280,2276,2276,2276,2276,2276,2276,2276,2272,2272,2272,2272,
  2272,2272,2272,2272,2272,2268,2268,2268,2268,2268,2268,2268,2264,2264,2264,2264,2264,2264,2264,2264,2264,2260,2260,2260,2260,2260,2260,2260,2256,2256,2256,2256,
  2256,2256,2256,2256,2256,2252,2252,2252,2252,2252,2252,2252,2248,2248,2248,2248,2248,2248,2248,2248,2248,2244,2244,2244,2244,2244,2244,2244,2240,2240,2240,2240,
  2240,2240,2240,2240,2240,2236,2236,2236,2236,2236,2236,2236,2232,2232,2232,2232,2232,2232,2232,2232,2232,2228,2228,2228,2228,2228,2228,2228,2224,2224,2224,2224,
  2224,2224,2224,2224,2224,2220,2220,2220,2220,2220,2220,2220,2216,2216,2216,2216,2216,2216,2216,2216,2216,2212,2212,2212,2212,2212,2212,2212,2208,2208,2208,2208,
  2208,2208,2208,2208,2208,2204,2204,2204,2204,2204,2204,2204,2200,2200,2200,2200,2200,2200,2200,2200,2200,2196,2196,2196,2196,2196,2196,2196,2192,2192,2192,2192,
  2192,2192,2192,2192,2192,2188,2188,2188,2188,2188,2188,2188,2184,2184,2184,2184,2184,2184,2184,2184,2184,2180,2180,2180,2180,2180,2180,2180,2176,2176,2176,2176,
  2176,2176,2176,2176,2176,2172,2172,2172,2172,2172,2172,2172,2168,2168,2168,2168,2168,2168,2168,2168,2168,2164,2164,2164,2164,2164,2164,2164,2160,2160,2160,2160,
  2160,2160,2160,2160,2160,2156,2156,2156,2156,2156,2156,2156,2152,2152,2152,2152,2152,2152,2152,2152,2152,2148,2148,2148,2148,2148,2148,2148,2144,2144,2144,2144,
  2144,2144,2144,2144,2144,2140,2140,2140,2140,2140,2140,2140,2136,2136,2136,2136,2136,2136,2136,2136,2136,2132,2132,2132,2132,2132,2132,2132,2128,2128,2128,2128,
  2128,2128,2128,2128,2128,2124,2124,2124,2124,2124,2124,2124,2120,2120,2120,2120,2120,2120,2120,2120,2120,2116,2116,2116,2116,2116,2116,2116,2112,2112,2112,2112,
  2112,2112,2112,2112,2112,2108,2108,2108,2108,2108,2108,2108,2104,2104,2104,2104,2104,2104,2104,2104,2104,2100,2100,2100,2100,2100,2100,2100,2096,2096,2096,2096,
  2096,2096,2096,2096,2096,2092,2092,2092,2092,2092,2092,2092,2088,2088,2088,2088,2088,2088,2088,2088,2088,2084,2084,2084,2084,2084,2084,2084,2080,2080,2080,2080,
  2080,2080,2080,2080,2080,2076,2076,2076,2076,2076,2076,2076,2072,2072,2072,2072,2072,2072,2072,2072,2072,2068,2068,2068,2068,2068,2068,2068,2064,2064,2064,2064,
  2064,2064,2064,2064,2064,2060,2060,2060,2060,2060,2060,2060,2056,2056,2056,2056,2056,2056,2056,2056,2056,2052,2052,2052,2052,2052,2052,2052,2048,2048,2048,2048,
  2048,2048,2048,2044,2044,2044,1916,1916,1916,1916,1916,2040,2040,2040,1912,1912,1912,1912,1912,2036,2036,2036,1908,1908,1908,1908,1908,2032,2032,2032,1904,1904,
  1904,1904,1904,2028,2028,2028,1900,1900,1900,1900,1900,2024,2024,2024,1896,1896,1896,1896,1896,2020,2020,2020,1892,1892,1892,1892,1892,2016,2016,2016,1888,1888,
  1888,1888,1888,2012,2012,2012,1884,1884,1884,1884,1884,2008,2008,2008,1880,1880,1880,1880,1880,2004,2004,2004,1876,1876,1876,1876,1876,2000,2000,2000,1872,1872,
  1872,1872,1872,1996,1996,1996,1868,1868,1868,1868,1868,1992,1992,1992,1864,1864,1864,1864,1864,1988,1988,1988,1860,1860,1860,1860,1860,1984,1984,1984,1856,1856,
  1856,1856,1856,1980,1980,1980,1852,1852,1852,1852,1852,1976,1976,1976,1848,1848,1848,1848,1848,1972,1972,1972,1844,1844,1844,1844,1844,1968,1968,1968,1840,1840,
  1840,1840,1840,1964,1964,1964,1836,1836,1836,1836,1836,1960,1960,1960,1832,1832,1832,1832,1832,1956,1956,1956,1828,1828,1828,1828,1828,1952,1952,1952,1824,1824,
  1824,1824,1824,1948,1948,1948,1820,1820,1820,1820,1820,1944,1944,1944,1816,1816,1816,1816,1816,1940,1940,1940,1812,1812,1812,1812,1812,1936,1936,1936,1808,1808,
  1808,1808,1808,1932,1932,1932,1804,1804,1804,1804,1804,1928,1928,1928,1800,1800,1800,1800,1800,1924,1924,1924,1796,1796,1796,1796,1796,1920,1920,1920,1792,1792,
  1792,1792,1792,1788,1788,1788,1660,1660,1660,1660,1660,1784,1784,1784,1656,1656,1656,1656,1656,1780,1780,1780,1652,1652,1652,1652,1652,1776,1776,1776,1648,1648,
  1648,1648,1648,1772,1772,1772,1644,1644,1644,1644,1644,1768,1768,1768,1640,1640,1640,1640,1640,1764,1764,1764,1636,1636,1636,1636,1636,1760,1760,1760,1632,1632,
  1632,1632,1632,1756,1756,1756,1628,1628,1628,1628,1628,1752,1752,1752,1624,1624,1624,1624,1624,1748,1748,1748,1620,1620,1620,1620,1620,1744,1744,1744,1616,1616,
  1616,1616,1616,1740,1740,1740,1612,1612,1612,1612,1612,1736,1736,1736,1608,1608,1608,1608,1608,1732,1732,1732,1604,1604,1604,1604,1604,1728,1728,1728,1600,1600,
  1600,1600,1600,1724,1724,1724,1596,1596,1596,1596,1596,1720,1720,1720,1592,1592,1592,1592,1592,1716,1716,1716,1588,1588,1588,1588,1588,1712,1712,1712,1584,1584,
  1584,1584,1584,1708,1708,1708,1580,1580,1580,1580,1580,1704,1704,1704,1576,1576,1576,1576,1576,1700,1700,1700,1572,1572,1572,1572,1572,1696,1696,1696,1568,1568,
  1568,1568,1568,1692,1692,1692,1564,1564,1564,1564,1564,1688,1688,1688,1560,1560,1560,1560,1560,1684,1684,1684,1556,1556,1556,1556,1556,1680,1680,1680,1552,1552,
  1552,1552,1552,1676,1676,1676,1548,1548,1548,1548,1548,1672,1672,1672,1544,1544,1544,1544,1544,1668,1668,1668,1540,1540,1540,1540,1540,1664,1664,1664,1536,1536,
  1536,1536,1532,1404,1404,1404,1276,1148,1148,1148,1528,1400,1400,1400,1272,1144,1144,1144,1524,1396,1396,1396,1268,1140,1140,1140,1520,1392,1392,1392,1264,1136,
  1136,1136,1516,1388,1388,1388,1260,1132,1132,1132,1512,1384,1384,1384,1256,1128,1128,1128,1508,1380,1380,1380,1252,1124,1124,1124,1504,1376,1376,1376,1248,1120,
  1120,1120,1500,1372,1372,1372,1244,1116,1116,1116,1496,1368,1368,1368,1240,1112,1112,1112,1492,1364,1364,1364,1236,1108,1108,1108,1488,1360,1360,1360,1232,1104,
  1104,1104,1484,1356,1356,1356,1228,1100,1100,1100,1480,1352,1352,1352,1224,1096,1096,1096,1476,1348,1348,1348,1220,1092,1092,1092,1472,1344,1344,1344,1216,1088,
  1088,1088,1468,1340,1340,1340,1212,1084,1084,1084,1464,1336,1336,1336,1208,1080,1080,1080,1460,1332,1332,1332,1204,1076,1076,1076,1456,1328,1328,1328,1200,1072,
  1072,1072,1452,1324,1324,1324,1196,1068,1068,1068,1448,1320,1320,1320,1192,1064,1064,1064,1444,1316,1316,1316,1188,1060,1060,1060,1440,1312,1312,1312,1184,1056,
  1056,1056,1436,1308,1308,1308,1180,1052,1052,1052,1432,1304,1304,1304,1176,1048,1048,1048,1428,1300,1300,1300,1172,1044,1044,1044,1424,1296,1296,1296,1168,1040,
  1040,1040,1420,1292,1292,1292,1164,1036,1036,1036,1416,1288,1288,1288,1160,1032,1032,1032,1412,1284,1284,1284,1156,1028,1028,1028,1408,1280,1280,1280,1152,1024,
  1024,1020,892,764,636,508,380,252,124,1016,888,760,632,504,376,248,120,1012,884,756,628,500,372,244,116,1008,880,752,624,496,368,240,
  112,1004,876,748,620,492,364,236,108,1000,872,744,616,488,360,232,104,996,868,740,612,484,356,228,100,992,864,736,608,480,352,224,
  96,988,860,732,604,476,348,220,92,984,856,728,600,472,344,216,88,980,852,724,596,468,340,212,84,976,848,720,592,464,336,208,
  80,972,844,716,588,460,332,204,76,968,840,712,584,456,328,200,72,964,836,708,580,452,324,196,68,960,832,704,576,448,320,192,
  64,956,828,700,572,444,316,188,60,952,824,696,568,440,312,184,56,948,820,692,564,436,308,180,52,944,816,688,560,432,304,176,
  48,940,812,684,556,428,300,172,44,936,808,680,552,424,296,168,40,932,804,676,548,420,292,164,36,928,800,672,544,416,288,160,
  32,924,796,668,540,412,284,156,28,920,792,664,536,408,280,152,24,916,788,660,532,404,276,148,20,912,784,656,528,400,272,144,
  16,908,780,652,524,396,268,140,12,904,776,648,520,392,264,136,8,900,772,644,516,388,260,132,4,896,768,640,512,384,256,128,
};

constexpr int KB_CODE_LINES = 56, KC_CODE_LINES = 296;
#define W1_POS(fl, n0, kstr) do { const int bk_ = fl >> 1; \
    n0 = bk_ < 4 ? (bk_ & 1) * 4 + (bk_ >> 1) * 2 + (fl & 1) : (bk_ < 8 ? 144 + ((bk_ - 4) >> 1) * 72 + (fl & 3) : 288 + (fl & 1)); \
    kstr = bk_ < 4 ? 8 : (bk_ < 8 ? 4 : 2); } while (0)
DI int w2_pos(int q, int j, int jm, int blk, int dtl) {
  const int ps = blk < 8 ? (blk >> 1) * 4 + ((jm - j) & 3) : 16 + (jm >> 1), pp = blk < 8 ? (blk & 1) : (jm & 1);
  const int ai = dtl == 8 ? 6 : ((dtl >> 1) == q ? 7 + (dtl & 1) : ((((dtl >> 1) - q - 1) & 3) * 2 + (dtl & 1)));
  const int sp = ai == 6 ? 0 : ai + 1;
  constexpr int KA_ = 8, KB_ = 15, NT = 3, ND = 10;
  const int n = ps < KA_ ? ps * 18 + ai * 2
              : (ai >= 7 ? 324 - 4 * ND + (ps - KA_) * 4 + (ai - 7) * 2
                         : (ps < KB_ ? KA_ * 18 + (ps - KA_) * 14 + ai * 2 : KA_ * 18 + (KB_ - KA_) * 14 + sp * 2 * NT + (ps - KB_) * 2));
  return n + pp;
}
constexpr int PFKB = 0;
constexpr int U1 = 1, U2 = 1, W2_IN_KA = 0, PRIO1 = 0, PRIO2 = 0, DUMMY = 0, WSLEEP = 0, W1FIRST = 0;
DI void w2_prep(const u16* __restrict__ w2, char* __restrict__ ws, char* smem, int b) {
  const int tid = threadIdx.x, lane = tid & 63, wave = __builtin_amdgcn_readfirstlane(tid >> 6);
  const int i16 = lane & 15, g = lane >> 4, q4 = i16 >> 2, p4 = i16 & 3;
  const auto rs = MKRSRC(ws, WS_END);
  __builtin_amdgcn_s_setprio(PRIO2);
  const u32x4* src = (const u32x4*)(w2 + (size_t)b * (32 * U2) * D_);
  u32x4 v[U2][9];
  const int row = tid >> 3, c8 = tid & 7;
#pragma unroll
  for (int h = 0; h < U2; ++h)
#pragma unroll
    for (int i = 0; i < 9; ++i) v[h][i] = src[(h * 32 + row) * 72 + c8 + 8 * i];
#pragma unroll
  for (int h = 0; h < U2; ++h) {
#pragma unroll
    for (int i = 0; i < 9; ++i) *(u32x4*)(smem + row * 1184 + (c8 + 8 * i) * 16) = v[h][i];
    __syncthreads();
    const int fb = b * U2 + h;
    const int q = fb / 36, fbl = fb - q * 36;
    const int jm = fbl / 9, blk = fbl - jm * 9;
    const u32 dst = WS_WST + ((q * 4 + wave) * 648 + 324) * 1024;
#pragma unroll
    for (int dtl = 0; dtl < 9; ++dtl) {
      const char* a = smem + (4 * g + q4) * 1184 + ((wave * 9 + dtl) * 16 + 4 * p4) * 2;
      u32x2 lo = tr_read(a), hi = tr_read(a + 16 * 1184);
      u32x4 o = {lo[0], lo[1], hi[0], hi[1]};
      ST16S(rs, lane * 16, dst + w2_pos(q, wave, jm, blk, dtl) * 1024, o);
    }
    if (h + 1 < U2) __syncthreads();
  }
}
constexpr int KA_LN = 64, KA_MOD = 108, KA_KG = 576, KA_W2 = W2_IN_KA ? 144 / U2 : 0;
constexpr int KA_LDS = 4 * 8 * 1216;

DI f32x2 cmulp(f32x2 a, f32x2 b, f32x2 bp) { return __builtin_elementwise_fma(a.yy, bp, a.xx * b); }
DI f32x2 cperp(f32x2 b) { return (f32x2){-b.y, b.x}; }
DI f32x2 cexp_p(float zr, float zi, float p) {
  const float mag = __builtin_amdgcn_exp2f(zr * p), ph = __builtin_amdgcn_fractf(zi * p);
  return (f32x2){mag * __builtin_amdgcn_cosf(ph), mag * __builtin_amdgcn_sinf(ph)};
}
DI u32 pkh2(f32x2 v) { return __builtin_bit_cast(u32, __builtin_convertvector(v, f16x2)); }
template <int CTRL> DI float dppf(float x) { return __int_as_float(__builtin_amdgcn_update_dpp(0, __float_as_int(x), CTRL, 0xf, 0xf, false)); }
DI float red8x8(const float (&a)[8], int lane) {
  u32 x[8];
#pragma unroll
  for (int j = 0; j < 8; ++j) x[j] = __float_as_uint(a[j]);
  swap32x4(x[0], x[4], x[1], x[5], x[2], x[6], x[3], x[7]);
  u32 y[4];
#pragma unroll
  for (int j = 0; j < 4; ++j) y[j] = __float_as_uint(__uint_as_float(x[j]) + __uint_as_float(x[j + 4]));
  swap16x2(y[0], y[2], y[1], y[3]);
  const float c[2] = {__uint_as_float(y[0]) + __uint_as_float(y[2]), __uint_as_float(y[1]) + __uint_as_float(y[3])};
  const bool hi = lane & 8;
  const float keep = hi ? c[1] : c[0], send = hi ? c[0] : c[1];
  return keep + dppf<0x128>(send);
}

extern "C" __global__ __launch_bounds__(256) void ka_prep(const u16* __restrict__ u, const u16* __restrict__ lar, const u16* __restrict__ aim,
                                               const u16* __restrict__ cre, const u16* __restrict__ cim, const u16* __restrict__ ldt,
                                               const u16* __restrict__ cvec, const u16* __restrict__ wmod, const u16* __restrict__ w2,
                                               char* __restrict__ ws) {
  __shared__ __attribute__((aligned(16))) char smem[KA_LDS];
  const int tid = threadIdx.x, lane = tid & 63, wave = tid >> 6;
  int b = blockIdx.x;
  const int i16 = lane & 15, g = lane >> 4, q4 = i16 >> 2, p4 = i16 & 3;
  const auto rs = MKRSRC(ws, WS_END);
  asm volatile("" :: "s"(u), "s"(lar), "s"(aim), "s"(cre), "s"(cim), "s"(ldt), "s"(cvec), "s"(wmod), "s"(w2), "s"(ws));
  b -= KA_KG;
  if (b >= 0 && b < KA_LN) {
    __builtin_amdgcn_s_setprio(3);
    const int r = lane >> 3, c = lane & 7;
    const int t = b * 32 + wave * 8 + r;
    const u32x4* src = (const u32x4*)(u + t * D_);
    u32x4 v[9];
#pragma unroll
    for (int i = 0; i < 9; ++i) v[i] = src[c + 8 * i];
    if (tid < 16) { const u32x4 z = {0u, 0u, 0u, 0u}; ST16(rs, WS_FLAG + (b * 16 + tid) * 16, z); }
    char* base = smem + wave * 8 * 1216;
    f32x2 s2 = {0.f, 0.f};
#pragma unroll
    for (int i = 0; i < 9; ++i) {
      *(u32x4*)(base + r * 1216 + (c + 8 * i) * 16) = v[i];
#pragma unroll
      for (int j = 0; j < 4; ++j) s2 += (f32x2){bf_lo(v[i][j]), bf_hi(v[i][j])};
    }
    __syncthreads();
    const u32 dst = WS_UT + (b * D_ + wave * 144 + i16) * 64 + g * 16;
#pragma unroll
    for (int it = 0; it < 9; ++it) {
      const char* a = smem + (g * 8 + q4) * 1216 + (wave * 144 + it * 16 + 4 * p4) * 2;
      u32x2 lo = tr_read(a), hi = tr_read(a + 4 * 1216);
      u32x4 o = {lo[0], lo[1], hi[0], hi[1]};
      ST16(rs, dst + it * 1024, o);
    }
    float s = s2[0] + s2[1];
    s += dppf<0xb1>(s); s += dppf<0x4e>(s); s += dppf<0x141>(s);
    const float mean = s * (1.f / D_);
    f32x2 q2 = {0.f, 0.f};
#pragma unroll
    for (int i = 0; i < 9; ++i)
#pragma unroll
      for (int j = 0; j < 4; ++j) {
        const f32x2 a = (f32x2){bf_lo(v[i][j]), bf_hi(v[i][j])} - mean;
        q2 = __builtin_elementwise_fma(a, a, q2);
      }
    float q = q2[0] + q2[1];
    q += dppf<0xb1>(q); q += dppf<0x4e>(q); q += dppf<0x141>(q);
    if (c == 0) {
      const float vb = rbf(q * (1.f / D_));
      const float sd = rbf(__builtin_amdgcn_sqrtf(rbf(vb + 1e-5f)));
      ST4(rs, WS_STATS + t * 4, pack2bf(mean, sd));
    }
    return;
  }
  if (W2_IN_KA && b >= KA_LN + KA_MOD) { w2_prep(w2, ws, smem, b - KA_LN - KA_MOD); return; }
  if (b >= KA_LN) {
    b -= KA_LN;
    __builtin_amdgcn_s_setprio(2);
    const int strip = b % 54, ks = b / 54;
    const int kr = tid >> 3, c8 = tid & 7;
    const int k0 = ks * 288 + kr;
    u32x4 w[9];
    u16 cw[9];
#pragma unroll
    for (int i = 0; i < 9; ++i) w[i] = *(const u32x4*)(wmod + (size_t)(k0 + 32 * i) * MODN + strip * 64 + c8 * 8);
#pragma unroll
    for (int i = 0; i < 9; ++i) cw[i] = cvec[k0 + 32 * i];
    u32 pfv = 0;
    if (b < 16 && tid < KB_CODE_LINES) CODE_PF(kb_conv, tid * 128, pfv);
    f32x2 acc[4];
#pragma unroll
    for (int j = 0; j < 4; ++j) acc[j] = (f32x2){0.f, 0.f};
#pragma unroll
    for (int i = 0; i < 9; ++i) {
      const float cv = bf2f(cw[i]);
      const float sc = rbf(cv * __builtin_amdgcn_rcpf(1.f + __builtin_amdgcn_exp2f(-LOG2E * cv)));
#pragma unroll
      for (int j = 0; j < 4; ++j) acc[j] = __builtin_elementwise_fma((f32x2){bf_lo(w[i][j]), bf_hi(w[i][j])}, (f32x2){sc, sc}, acc[j]);
    }
    const float a8[8] = {acc[0][0], acc[0][1], acc[1][0], acc[1][1], acc[2][0], acc[2][1], acc[3][0], acc[3][1]};
    const float tot = red8x8(a8, lane);
    float* red = (float*)smem;
    red[wave * 64 + c8 * 8 + (lane >> 3)] = tot;
    __syncthreads();
    if (tid < 64) ST4(rs, WS_MODP + (ks * MODN + strip * 64 + tid) * 4, __float_as_uint(add4(red[tid], red[64 + tid], red[128 + tid], red[192 + tid])));
    asm volatile("" :: "v"(pfv));
    return;
  }
  {
    const int d = b + KA_KG;
    const int n = tid & 127, role = __builtin_amdgcn_readfirstlane(tid >> 7);
    u32 vla = lar[d * N_ + n], vai = aim[d * N_ + n], vcr = cre[d * N_ + n], vci = cim[d * N_ + n], vdt = ldt[d];
    const u32x4 tab = *(const u32x4*)(KIDX + tid * 8);
    asm volatile("" : "+v"(vla), "+v"(vai), "+v"(vcr), "+v"(vci), "+v"(vdt));
    const float dt = rbf(__builtin_amdgcn_exp2f(bf_lo(vdt) * LOG2E));
    const float zr = -rbf(__builtin_amdgcn_exp2f(bf_lo(vla) * LOG2E)) * dt * LOG2E;
    const float zi = bf_lo(vai) * dt * 0.15915494309189535f;
    constexpr int IP = 528;
    char* imgA = smem;
    char* imgB = smem + 32 * IP;
    float* scl = (float*)(smem + 64 * IP);
    float* kd = (float*)(smem + 64 * IP + 16);
    if (role == 0) {
      const f32x2 e8 = cexp_p(zr, zi, 8.f), e64 = cexp_p(zr, zi, 64.f);
      const f32x2 e8p = cperp(e8), e64p = cperp(e64);
      f32x2 w[4];
      w[0] = (f32x2){1.f, 0.f}; w[1] = e64; w[2] = cmulp(e64, e64, e64p); w[3] = cmulp(w[2], e64, e64p);
      char* p = imgB + n * 4;
#pragma unroll
      for (int s = 0; s < 8; ++s) {
#pragma unroll
        for (int c = 0; c < 4; ++c) {
          *(u32*)(p + (c * 8 + s) * IP) = pkh2(w[c]);
          if (s < 7) w[c] = cmulp(w[c], e8, e8p);
        }
      }
    } else {
      const float c_r = bf_lo(vcr), c_i = -bf_lo(vci);
      float cm = fmaxf(fabsf(c_r), fabsf(c_i));
      cm = fmaxf(cm, dppf<0xb1>(cm)); cm = fmaxf(cm, dppf<0x4e>(cm)); cm = fmaxf(cm, dppf<0x141>(cm)); cm = fmaxf(cm, dppf<0x140>(cm));
      { u32 p = __float_as_uint(cm), r = p; swap16(p, r); cm = fmaxf(__uint_as_float(p), __uint_as_float(r)); }
      { u32 p = __float_as_uint(cm), r = p; swap32(p, r); cm = fmaxf(__uint_as_float(p), __uint_as_float(r)); }
      const u32 ce = __float_as_uint(cm) & 0x7f800000u;
      const float cs = ce ? __uint_as_float(ce) : 1.f;
      const float cinv = __builtin_amdgcn_rcpf(cs);
      if (lane == 0) scl[wave & 1] = 2.f * dt * cs;
      const f32x2 C = {c_r * cinv, c_i * cinv};
      const f32x2 e1 = cexp_p(zr, -zi, 1.f), e256 = cexp_p(zr, -zi, 256.f);
      const f32x2 e1p = cperp(e1), e256p = cperp(e256);
      const f32x2 e2 = cmulp(e1, e1, e1p), e2p = cperp(e2), e4 = cmulp(e2, e2, e2p), e4p = cperp(e4);
      char* p = imgA + n * 4;
      auto put = [&](int row, f32x2 v) { *(u32*)(p + row * IP) = pkh2(v); };
      f32x2 a = C, a4 = cmulp(C, e4, e4p), P = cmulp(C, e256, e256p);
#pragma unroll
      for (int s = 0; s < 4; ++s) { put(s, a); put(4 + s, a4); if (s < 3) { a = cmulp(a, e1, e1p); a4 = cmulp(a4, e1, e1p); } }
      { put(8, P); put(9, cmulp(P, e2, e2p)); const f32x2 x = cmulp(P, e4, e4p); put(10, x); put(11, cmulp(x, e2, e2p)); }
      P = cmulp(P, e256, e256p); put(12, P); put(13, cmulp(P, e4, e4p));
      P = cmulp(P, e256, e256p); put(14, P); put(15, cmulp(P, e4, e4p));
#pragma unroll
      for (int s = 0; s < 5; ++s) { P = cmulp(P, e256, e256p); put(16 + s, P); }
    }
    __syncthreads();
    {
      const int rh = wave >> 1, ch = wave & 1;
      const char* pa = imgA + (rh * 16 + i16) * IP + g * 16;
      const char* pb = imgB + (ch * 16 + i16) * IP + g * 16;
      f32x4 acc0 = {0.f, 0.f, 0.f, 0.f}, acc1 = {0.f, 0.f, 0.f, 0.f};
#pragma unroll
      for (int s = 0; s < 4; ++s) {
        acc0 = mfma_f16(*(const u32x4*)(pa + s * 64), *(const u32x4*)(pb + s * 64), acc0);
        acc1 = mfma_f16(*(const u32x4*)(pa + 256 + s * 64), *(const u32x4*)(pb + 256 + s * 64), acc1);
      }
      const float s0 = scl[0], s1 = scl[1];
#pragma unroll
      for (int r = 0; r < 4; ++r) kd[(rh * 16 + 4 * g + r) * 32 + ch * 16 + i16] = acc0[r] * s0 + acc1[r] * s1;
    }
    __syncthreads();
    u32x4 o;
#pragma unroll
    for (int e = 0; e < 4; ++e) o[e] = pack2bf(*(const float*)((const char*)kd + (tab[e] & 0xffffu)), *(const float*)((const char*)kd + (tab[e] >> 16)));
    ST16(rs, WS_KR + d * (KRP * 2) + tid * 16, o);
    if (tid == 0) {
      const u32x4 z = {pack2bf(kd[0], 0.f), 0u, 0u, 0u};
      ST16(rs, WS_KR + d * (KRP * 2) + 4096, z);
    }
  }
}

constexpr int KB_W1 = 216 / U1, KB_W2 = W2_IN_KA ? 0 : 144 / U2, KB_CONV = 576;
constexpr int KB_DUM = DUMMY;
constexpr int CP = 4160;
constexpr int KB_HOFF = 8 * CP;
constexpr int KB_LDS = 37888;

extern "C" __global__ __launch_bounds__(256, 4) void kb_conv(const u16* __restrict__ dskip, const u16* __restrict__ w1, const u16* __restrict__ w2,
                                                             char* __restrict__ ws) {
  __shared__ __attribute__((aligned(16))) char smem[KB_LDS];
  const int tid = threadIdx.x, lane = tid & 63, wave = __builtin_amdgcn_readfirstlane(tid >> 6);
  const int i16 = lane & 15, g = lane >> 4, q4 = i16 >> 2, p4 = i16 & 3;
  const float* modp = (const float*)(ws + WS_MODP);
  const auto rs = MKRSRC(ws, WS_END);
  int b = blockIdx.x;
  asm volatile("" :: "s"(dskip), "s"(w1), "s"(w2), "s"(ws));
  if (KB_DUM && b >= 768) { if (b < 768 + KB_DUM) { return; } b -= KB_DUM; }
  if (W1FIRST && b >= KB_CONV) b = b < KB_CONV + KB_W1 ? b + KB_W2 : b - KB_W1;
  if (WSLEEP && b >= KB_CONV) __builtin_amdgcn_s_sleep(WSLEEP);
  if (b >= KB_CONV + KB_W2) {
    b -= KB_CONV + KB_W2;
    __builtin_amdgcn_s_setprio(PRIO1);
    const int fp = b % (72 / U1), ks = b / (72 / U1);
    const int kr = tid >> 3, c8 = tid & 7;
    const int k0 = ks * 192;
    u32x4 w[U1][6];
#pragma unroll
    for (int h = 0; h < U1; ++h)
#pragma unroll
      for (int i = 0; i < 6; ++i) w[h][i] = *(const u32x4*)(w1 + (size_t)(k0 + kr + 32 * i) * F_ + (fp * U1 + h) * 64 + c8 * 8);
    u32 pfv = 0;
    if (b < 16 && (b >> 3) * 256 + tid < KC_CODE_LINES) CODE_PF(kc_mlp, ((b >> 3) * 256 + tid) * 128, pfv);
    float* sm = (float*)(smem + 192 * 160);
    if (tid < 192) {
      f32x2 m = {rbf(1.f + modval(modp, 4 * D_ + k0 + tid)), modval(modp, 3 * D_ + k0 + tid)};
      *(f32x2*)(sm + tid * 2) = m;
    }
    __syncthreads();
    f32x2 m[6];
#pragma unroll
    for (int i = 0; i < 6; ++i) m[i] = *(f32x2*)(sm + (kr + 32 * i) * 2);
#pragma unroll
    for (int h = 0; h < U1; ++h) {
      const int fs = fp * U1 + h;
      f32x2 a2[4];
#pragma unroll
      for (int i = 0; i < 6; ++i) {
        u32x4 o;
#pragma unroll
        for (int j = 0; j < 4; ++j) {
          const f32x2 x = {bf_lo(w[h][i][j]), bf_hi(w[h][i][j])};
          const f32x2 y = m[i].xx * x;
          o[j] = pack2bf(y[0], y[1]);
          a2[j] = i ? __builtin_elementwise_fma(m[i].yy, x, a2[j]) : m[i].yy * x;
        }
        *(u32x4*)(smem + (kr + 32 * i) * 160 + c8 * 16) = o;
      }
      const float b2[8] = {a2[0][0], a2[0][1], a2[1][0], a2[1][1], a2[2][0], a2[2][1], a2[3][0], a2[3][1]};
      float* red = sm + 384;
      red[wave * 64 + c8 * 8 + (lane >> 3)] = red8x8(b2, lane);
      __syncthreads();
      if (tid < 64) {
        const float s = add4(red[tid], red[64 + tid], red[128 + tid], red[192 + tid]);
        ST4(rs, WS_VP + ((ks * 2 + 1) * F_ + fs * 64 + tid) * 4, __float_as_uint(s));
      }
      const int ft = fs * 4 + wave, cj = ft / 18, fl = ft - cj * 18;
      int n0, kstr;
      W1_POS(fl, n0, kstr);
      const u32 dst = WS_WST + (cj * 648 + n0 + ks * 6 * kstr) * 1024;
      f32x4 cs = {0.f, 0.f, 0.f, 0.f};
#pragma unroll
      for (int kk = 0; kk < 6; ++kk) {
        const char* a = smem + (kk * 32 + 4 * g + q4) * 160 + (wave * 16 + 4 * p4) * 2;
        u32x2 lo = tr_read(a), hi = tr_read(a + 16 * 160);
        u32x4 o = {lo[0], lo[1], hi[0], hi[1]};
        ST16S(rs, lane * 16, dst + kk * kstr * 1024, o);
        cs = mfma_bf16(o, (u32x4){0x3f803f80u, 0x3f803f80u, 0x3f803f80u, 0x3f803f80u}, cs);
      }
      if (i16 == 0) ST16(rs, WS_VP + (ks * 2 * F_ + ft * 16 + 4 * g) * 4, __builtin_bit_cast(u32x4, cs));
      if (h + 1 < U1) __syncthreads();
    }
    asm volatile("" :: "v"(pfv));
    return;
  }
  if (b >= KB_CONV) {
    w2_prep(w2, ws, smem, b - KB_CONV);
    return;
  }
  const int d = (b & 7) * 72 + (b >> 3);
  const u32x4 uv = *(const u32x4*)(ws + WS_UT + ((tid >> 2) * D_ + d) * 64 + (tid & 3) * 16);
  const u32x4* krp = (const u32x4*)(ws + WS_KR + d * (KRP * 2)) + tid;
  const u32x4 k0v = krp[0], k1v = krp[1];
  const u32x4 st0 = *((const u32x4*)(ws + WS_STATS) + tid * 2), st1 = *((const u32x4*)(ws + WS_STATS) + tid * 2 + 1);
  if (PFKB && b < 512) {
    const u32x4 sv = {~0u, ~0u, ~0u, ~0u};
#pragma unroll
    for (int i = 0; i < 4; ++i) ST16S(rs, tid * 16, WS_PART + (b * 4 + i) * 4096, sv);
  }
  const float sa = modval(modp, d), opc = rbf(1.f + modval(modp, D_ + d)), ga = modval(modp, 2 * D_ + d);
  const float gd = ga * bf2f(dskip[d]);
  if (tid == 0) ST4(rs, WS_GB + d * 4, __float_as_uint(modval(modp, 5 * D_ + d)));
  u32x4 hv;
  {
    const u32 w[8] = {k0v[0], k0v[1], k0v[2], k0v[3], k1v[0], k1v[1], k1v[2], k1v[3]};
    u32 o[7];
#pragma unroll
    for (int k = 0; k < 7; ++k) o[k] = __builtin_amdgcn_alignbit(w[k + 1], w[k], 16);
    char* dst = smem + (tid + 1) * 16;
#pragma unroll
    for (int q = 0; q < 8; ++q) {
      u32x4 v;
      if ((q & 1) == 0) { const int s = 4 - q / 2; v = (u32x4){w[s], w[s + 1], w[s + 2], w[s + 3]}; }
      else { const int s = (7 - q) / 2; v = (u32x4){o[s], o[s + 1], o[s + 2], o[s + 3]}; }
      *(u32x4*)(dst + q * CP) = v;
    }
    const u32x4 z = {0u, 0u, 0u, 0u};
    if (tid < 38) *(u32x4*)(smem + (tid < 30 ? KB_HOFF + tid * 16 : (tid - 30) * CP + 257 * 16)) = z;
#pragma unroll
    for (int j = 0; j < 4; ++j) {
      const u32 s0 = j < 2 ? st0[2 * j] : st1[2 * j - 4], s1 = j < 2 ? st0[2 * j + 1] : st1[2 * j - 3];
      const float a0 = opc * __builtin_amdgcn_rcpf(bf_hi(s0)), a1 = opc * __builtin_amdgcn_rcpf(bf_hi(s1));
      hv[j] = pack2bf(__builtin_fmaf(bf_lo(uv[j]) - bf_lo(s0), a0, sa), __builtin_fmaf(bf_hi(uv[j]) - bf_lo(s1), a1, sa));
    }
    *(u32x4*)(smem + KB_HOFF + (30 + tid) * 16) = hv;
  }
  __syncthreads();
  f32x4 acc[8];
  {
    const char* abase = smem + (i16 & 7) * CP + (254 + g - (i16 >> 3)) * 16 - (wave * 2 + 57) * 64;
    const char* bbase = smem + KB_HOFF + (28 + 2 * i16 + g) * 16 - (wave * 2 + 1) * 64;
#define CA(rr, m) const u32x4 A##rr##_##m = *(const u32x4*)(abase + (57 - rr - 8 * m) * 64)
#define CB(rr, n) const u32x4 B##rr##_##n = *(const u32x4*)(bbase + (8 * n + 1 - rr) * 64)
#define CM0(c, a, b) asm volatile("v_mfma_f32_16x16x32_bf16 %0, %1, %2, 0" : "=v"(acc[c]) : "v"(a), "v"(b)); SCHED()
#define CM(c, a, b) asm volatile("v_mfma_f32_16x16x32_bf16 %0, %1, %2, %0" : "+v"(acc[c]) : "v"(a), "v"(b)); SCHED()
    CA(0, 0);
    CB(0, 0);
    CB(0, 1);
    CB(0, 2);
    CB(0, 3);
    CB(0, 4);
    CB(0, 5);
    CB(0, 6);
    CB(0, 7);
    CA(0, 1);
    CM0(0, A0_0, B0_0);
    CM0(1, A0_0, B0_1);
    CM0(2, A0_0, B0_2);
    CM0(3, A0_0, B0_3);
    CM0(4, A0_0, B0_4);
    CM0(5, A0_0, B0_5);
    CM0(6, A0_0, B0_6);
    CA(0, 2);
    CM0(7, A0_0, B0_7);
    CM(1, A0_1, B0_0);
    CM(2, A0_1, B0_1);
    CM(3, A0_1, B0_2);
    CM(4, A0_1, B0_3);
    CM(5, A0_1, B0_4);
    CA(0, 3);
    CM(6, A0_1, B0_5);
    CM(7, A0_1, B0_6);
    CM(2, A0_2, B0_0);
    CM(3, A0_2, B0_1);
    CM(4, A0_2, B0_2);
    CA(0, 4);
    CM(5, A0_2, B0_3);
    CM(6, A0_2, B0_4);
    CM(7, A0_2, B0_5);
    CM(3, A0_3, B0_0);
    CA(0, 5);
    CM(4, A0_3, B0_1);
    CM(5, A0_3, B0_2);
    CM(6, A0_3, B0_3);
    CA(0, 6);
    CM(7, A0_3, B0_4);
    CA(0, 7);
    CM(4, A0_4, B0_0);
    CB(1, 0);
    CM(5, A0_4, B0_1);
    CA(1, 0);
    CM(6, A0_4, B0_2);
    CB(1, 1);
    CM(7, A0_4, B0_3);
    CB(1, 2);
    CM(5, A0_5, B0_0);
    CB(1, 3);
    CM(6, A0_5, B0_1);
    CB(1, 4);
    CM(7, A0_5, B0_2);
    CB(1, 5);
    CM(6, A0_6, B0_0);
    CB(1, 6);
    CM(7, A0_6, B0_1);
    CB(1, 7);
    CM(7, A0_7, B0_0);
    CA(1, 1);
    CM(0, A1_0, B1_0);
    CM(1, A1_0, B1_1);
    CM(2, A1_0, B1_2);
    CM(3, A1_0, B1_3);
    CM(4, A1_0, B1_4);
    CM(5, A1_0, B1_5);
    CM(6, A1_0, B1_6);
    CA(1, 2);
    CM(7, A1_0, B1_7);
    CM(1, A1_1, B1_0);
    CM(2, A1_1, B1_1);
    CM(3, A1_1, B1_2);
    CM(4, A1_1, B1_3);
    CM(5, A1_1, B1_4);
    CA(1, 3);
    CM(6, A1_1, B1_5);
    CM(7, A1_1, B1_6);
    CM(2, A1_2, B1_0);
    CM(3, A1_2, B1_1);
    CM(4, A1_2, B1_2);
    CA(1, 4);
    CM(5, A1_2, B1_3);
    CM(6, A1_2, B1_4);
    CM(7, A1_2, B1_5);
    CM(3, A1_3, B1_0);
    CA(1, 5);
    CM(4, A1_3, B1_1);
    CM(5, A1_3, B1_2);
    CM(6, A1_3, B1_3);
    CA(1, 6);
    CM(7, A1_3, B1_4);
    CM(4, A1_4, B1_0);
    CA(1, 7);
    CM(5, A1_4, B1_1);
    CM(6, A1_4, B1_2);
    CM(7, A1_4, B1_3);
    CM(5, A1_5, B1_0);
    CM(6, A1_5, B1_1);
    CM(7, A1_5, B1_2);
    CM(6, A1_6, B1_0);
    CM(7, A1_6, B1_1);
    CM(7, A1_7, B1_0);
    asm volatile("s_nop 7\n\ts_nop 3" ::: "memory"); SCHED();
  }
  __syncthreads();
  {
    f32x4* red = (f32x4*)smem;
    const int wsl = (g & 1) * 32 + 2 * i16 + (g >> 1);
#pragma unroll
    for (int c = 0; c < 8; ++c) red[(wave * 8 + c) * 64 + wsl] = acc[c];
    __syncthreads();
    const f32x4* rp = red + (tid >> 5) * 64 + (tid & 31);
    u32x4 ov;
#pragma unroll
    for (int e = 0; e < 2; ++e) {
      const f32x4 y = (rp[e * 32] + rp[512 + e * 32]) + (rp[1024 + e * 32] + rp[1536 + e * 32]);
#pragma unroll
      for (int k = 0; k < 2; ++k) {
        const u32 hh = hv[2 * e + k], uu = uv[2 * e + k];
        ov[2 * e + k] = pack2bf(__builtin_fmaf(y[2 * k], ga, __builtin_fmaf(bf_lo(hh), gd, bf_lo(uu))),
                                __builtin_fmaf(y[2 * k + 1], ga, __builtin_fmaf(bf_hi(hh), gd, bf_hi(uu))));
      }
    }
    ST16(rs, WS_X1S + ((tid >> 4) * D_ + d) * 256 + (((tid & 15) ^ ((d & 7) << 1)) * 16), ov);
  }
}

constexpr int KC_MISC = 18 * 8192;
constexpr int KC_LDS = KC_MISC + 16384;
#define LAS __attribute__((address_space(3)))
#define MFMA0(d, a, b) asm volatile("v_mfma_f32_16x16x32_bf16 %0, %1, %2, 0" : "=v"(d) : "v"(a), "v"(b))
#define MFMA(d, a, b) asm volatile("v_mfma_f32_16x16x32_bf16 %0, %1, %2, %0" : "+v"(d) : "v"(a), "v"(b))
#define DMA1(voff, soff, lds) asm volatile("s_mov_b32 m0, %0\n\ts_nop 0\n\tbuffer_load_dwordx4 %1, %2, %3 offen lds" \
    :: "s"(lds), "v"(voff), "s"(rs4), "s"(soff) : "memory")
#define DMA2(sb, simm, limm) do { u32 t_; asm volatile("s_add_u32 m0, %1, %2\n\ts_add_u32 %0, %3, %4\n\t" \
    "buffer_load_dwordx4 %5, %6, %0 offen lds\n\tbuffer_load_dwordx4 %5, %6, %0 offen offset:1024 lds" \
    : "=&s"(t_) : "s"(lbase), "i"(limm), "s"(sb), "i"(simm), "v"(l16), "s"(rs4) : "memory", "scc"); } while (0)
#define OPQ(x) asm volatile("" : "+v"(x))
#define OPQS(x) asm volatile("" : "+s"(x))
#define LDSR(base, off) (*(const LAS u32x4*)(uintptr_t)((base) + (off)))
#define LDSRF(base, off) (*(const LAS f32x4*)(uintptr_t)((base) + (off)))
#define LDSR2(base, off) (*(const LAS f32x2*)(uintptr_t)((base) + (off)))
#define LDSW2(base, off, v) (*(LAS f32x2*)(uintptr_t)((base) + (off)) = (v))
#define LDSWF(base, off, v) (*(LAS f32x4*)(uintptr_t)((base) + (off)) = (v))
#define TRR(base, off) __builtin_bit_cast(u32x2, __builtin_amdgcn_ds_read_tr16_b64_v4i16((lds_s16x4_ptr)(uintptr_t)((base) + (off))))
#define V_FMA(d, a, b, c) asm volatile("v_fma_f32 %0, %1, %2, %3" : "=v"(d) : "v"(a), "v"(b), "v"(c))
#define V_FMAC(d, a, b) asm volatile("v_fmac_f32 %0, %1, %2" : "+v"(d) : "v"(a), "v"(b))
#define V_EXP(d, a) asm volatile("v_exp_f32 %0, %1" : "=v"(d) : "v"(a))
#define V_ADD1(d) asm volatile("v_add_f32 %0, 1.0, %0" : "+v"(d))
#define V_RCP(d) asm volatile("v_rcp_f32 %0, %0" : "+v"(d))
#define V_MUL(d, a) asm volatile("v_mul_f32 %0, %0, %1" : "+v"(d) : "v"(a))
#define V_CVT(d, a, b) asm volatile("v_cvt_pk_bf16_f32 %0, %1, %2" : "=v"(d) : "v"(a), "v"(b))
#define V_MULC(d, a, c) asm volatile("v_mul_f32 %0, %1, %2" : "=v"(d) : "s"(c), "v"(a))
#define STAT_DIAG(d) do { float a_, b_; asm volatile("v_and_b32 %0, 16, %6\n\tv_cmp_eq_u32 vcc, 0, %0\n\tv_and_b32 %2, 32, %6\n\ts_nop 1\n\t" \
      "v_cndmask_b32 %0, %4, %3, vcc\n\tv_cndmask_b32 %1, %7, %5, vcc\n\tv_cmp_eq_u32 vcc, 0, %2\n\ts_nop 1\n\tv_cndmask_b32 %2, %1, %0, vcc" \
      : "=&v"(a_), "=&v"(b_), "=&v"(d) : "v"(stQ[0]), "v"(stQ[1]), "v"(stQ[2]), "v"(l16), "v"(stQ[3]) : "vcc"); } while (0)
#define STAT_BCAST(d) do { u32 ln_ = l16; OPQ(ln_); d = __int_as_float(__builtin_amdgcn_ds_bpermute((int)((ln_ & 0xc0u) | ((ln_ >> 2) & 0x3cu)), __float_as_int(d))); } while (0)
#define XADD(a, e) asm volatile("v_add_f32 %0, %1, %2" : "=v"(xs##a##_##e) : "v"(o##a##_0[e]), "v"(xo##a[e]))
#define XPACK(a) asm volatile("v_cvt_pk_bf16_f32 %0, %2, %3\n\tv_cvt_pk_bf16_f32 %1, %4, %5\n\tv_min_u32 %0, -2, %0\n\tv_min_u32 %1, -2, %1" \
      : "=&v"(pp##a##_0), "=&v"(pp##a##_1) : "v"(xs##a##_0), "v"(xs##a##_1), "v"(xs##a##_2), "v"(xs##a##_3))
#define XST(k, a, b) do { const u32x4 p_ = {pp##a##_0, pp##a##_1, pp##b##_0, pp##b##_1}; HST(PSLOT((q + 1 + (k)) & 3, 2 - (k), wave), p_); } while (0)
#define XST8() do { const u32x4 p_ = {pp6_0, pp6_1, pp6_0, pp6_1}; HST(sP8, p_); } while (0)
#define HLOAD(d, soff) asm volatile("buffer_load_dwordx4 %0, %1, %2, %3 offen sc1" : "=v"(d) : "v"(l16), "s"(rs4), "s"(soff) : "memory")
#define HST(soff, v) asm volatile("buffer_store_dwordx4 %0, %1, %2, %3 offen sc1\n\ts_nop 1" :: "v"(v), "v"(l16), "s"(rs4), "s"(soff) : "memory")
#define POLL(d, sbase, sh) do { u32 fo_; asm volatile("v_and_b32 %0, 48, %1\n\tv_lshlrev_b32 %0, " #sh ", %0" : "=v"(fo_) : "v"(l16)); \
    asm volatile("buffer_load_dword %0, %1, %2, %3 offen sc1" : "=v"(d) : "v"(fo_), "s"(rs4), "s"(sbase) : "memory"); } while (0)
#define CHECK(f, lvl, sbase, sh) do { if (!__all((f) >= (lvl))) { \
    u32 fo_ = l16; OPQ(fo_); \
    gu32* fp_ = (gu32*)(ws + (sbase) + ((fo_ & 48u) << (sh))); \
    _Pragma("nounroll") for (int spin_ = 0; spin_ < (1 << 20); ++spin_) { \
      const u32 f_ = __hip_atomic_load(fp_, __ATOMIC_RELAXED, __HIP_MEMORY_SCOPE_AGENT); \
      if (__all(f_ >= (lvl))) break; \
      __builtin_amdgcn_s_sleep(1); } } } while (0)
#define VSUM() do { if (tid < 144) { const f32x4* p_ = (const f32x4*)(smem + KC_MISC); \
    f32x4 v_ = p_[tid] + p_[144 + tid] + p_[288 + tid]; if (tid >= 72) v_ = v_ * (-LOG2E); \
    *(f32x4*)(smem + KC_MISC + 8192 + tid * 16) = v_; } } while (0)
#define WIN(name, k) u32 name##_##k = name##_0 + (k) * 65536; OPQ(name##_##k)
#define PSLOT(owner, k, w) (sPT + (((owner) * 4 + (k)) * 8 + (w)) * 1024)
#define PREFILL(k) do { u32x4 s_ = {~0u, ~0u, ~0u, ~0u}; OPQ(s_); HST((k) < 3 ? PSLOT((q + 1 + (k)) & 3, 2 - (k), wave) : sP8, s_); } while (0)
#define XRES0() DMA2(sXR, 0, KC_MISC)
#define XRES1() DMA1(l16, sXR8, lds0 + KC_MISC + 8192 + rg * 1024)
#define GBLOAD() do { u32 go_; asm volatile("v_lshrrev_b32 %0, 4, %1\n\tv_and_b32 %0, 48, %0" : "=v"(go_) : "v"(l16)); \
    asm volatile("buffer_load_dwordx4 %0, %3, %4, %5 offen\n\tbuffer_load_dwordx4 %1, %3, %4, %5 offen offset:64\n\tbuffer_load_dwordx4 %2, %3, %4, %6 offen" \
      : "=&v"(gb0), "=&v"(gb1), "=&v"(gb2) : "v"(go_), "s"(rs4), "s"(sGB), "s"(sGB8) : "memory"); } while (0)

DI u32 pk_part(float a, float b) { const u32 p = pack2bf(a, b); return p < 0xfffffffeu ? p : 0xfffffffeu; }
DI u32x2 fin4(f32x4 s, u32x2 xv, u32x4 gbv) {
  u32x2 o;
#pragma unroll
  for (int e = 0; e < 2; ++e) {
    const u32 sb = pack2bf(s[2 * e] * (-LN2), s[2 * e + 1] * (-LN2));
    const u32 g0 = gbv[2 * e], g1 = gbv[2 * e + 1];
    const u32 gs = pack2bf(bf_lo(sb) * __uint_as_float(g0), bf_hi(sb) * __uint_as_float(g1));
    o[e] = pack2bf(bf_lo(xv[e]) + bf_lo(gs), bf_hi(xv[e]) + bf_hi(gs));
  }
  return o;
}

#define PLOAD() asm volatile("buffer_load_dwordx4 %0, %6, %7, %8 offen sc1\n\tbuffer_load_dwordx4 %1, %6, %7, %9 offen sc1\n\tbuffer_load_dwordx4 %2, %6, %7, %10 offen sc1\n\t" \
                 "buffer_load_dwordx4 %3, %6, %7, %11 offen sc1\n\tbuffer_load_dwordx4 %4, %6, %7, %11 offen offset:2048 sc1\n\tbuffer_load_dwordx4 %5, %6, %7, %12 offen sc1" \
      : "=&v"(r0), "=&v"(r1), "=&v"(r2), "=&v"(r3), "=&v"(r4), "=&v"(r5) \
      : "v"(l16), "s"(rs4), "s"(PSLOT(q, 0, wave)), "s"(PSLOT(q, 1, wave)), "s"(PSLOT(q, 2, wave)), "s"(sR8), "s"(sR8 + 4096) : "memory")
#define TAIL(sk, w7, w8, r7, r8) do { \
  asm volatile("s_nop 7\n\ts_nop 7" ::: "memory"); SCHED(); \
  LDSWF(pE_0 + (w7), 0, o7_1); LDSWF(pE_0 + (w8), 0, o8_1); \
  WAITL0(); BAR(); \
  f32x4 s7 = o7_0 + LDSRF(pE_0 + (r7), 0), s8 = o8_0 + LDSRF(pE_0 + (r8), 0); \
  f32x4 s6 = {xs6_0, xs6_1, xs6_2, xs6_3}; \
  const u32 pxr = pX0_0 + KC_MISC; \
  const u32x2 x7 = TRR(pxr, 0), x8 = TRR(pxr, 4096), x6 = TRR(pxr, 8192); \
  asm volatile("s_waitcnt vmcnt(0)" : "+v"(gb0), "+v"(gb1), "+v"(gb2), "+v"(r0), "+v"(r1), "+v"(r2), "+v"(r3), "+v"(r4), "+v"(r5)::"memory"); \
  _Pragma("nounroll") for (int spin_ = 0; spin_ < (1 << 16); ++spin_) { \
    u32 m_ = max(max(max(r0[0], r0[1]), max(r0[2], r0[3])), max(max(r1[0], r1[1]), max(r1[2], r1[3]))); \
    m_ = max(m_, max(max(r2[0], r2[1]), max(r2[2], r2[3]))); \
    const u32 m8_ = max(max(max(r3[0], r3[1]), max(r4[0], r4[1])), max(r5[0], r5[1])); \
    if (rg == q) m_ = max(m_, m8_); \
    if (__all(m_ != ~0u)) break; \
    PLOAD(); \
    asm volatile("s_waitcnt vmcnt(0)" : "+v"(r0), "+v"(r1), "+v"(r2), "+v"(r3), "+v"(r4), "+v"(r5)::"memory"); } \
  s7 += (f32x4){bf_lo(r0[0]) + bf_lo(r1[0]) + bf_lo(r2[0]), bf_hi(r0[0]) + bf_hi(r1[0]) + bf_hi(r2[0]), bf_lo(r0[1]) + bf_lo(r1[1]) + bf_lo(r2[1]), bf_hi(r0[1]) + bf_hi(r1[1]) + bf_hi(r2[1])}; \
  s8 += (f32x4){bf_lo(r0[2]) + bf_lo(r1[2]) + bf_lo(r2[2]), bf_hi(r0[2]) + bf_hi(r1[2]) + bf_hi(r2[2]), bf_lo(r0[3]) + bf_lo(r1[3]) + bf_lo(r2[3]), bf_hi(r0[3]) + bf_hi(r1[3]) + bf_hi(r2[3])}; \
  const u32x2 f7 = fin4(s7, x7, gb0), f8 = fin4(s8, x8, gb1); \
  u32 fa0 = f7[0], fb0 = f8[0], fa1 = f7[1], fb1 = f8[1]; \
  swap16x2(fa0, fb0, fa1, fb1); \
  const u32x4 ov = {fa0, fa1, fb0, fb1}; \
  u32 ln_ = l16; OPQ(ln_); const u32 gg = ln_ >> 8, tl = (ln_ >> 4) & 15u; \
  const auto ro = MKRSRC(out, L_ * D_ * 2); \
  const u32 orow = ((rt * 8 + T0) * 16 + tl) * (D_ * 2) + j * 288; \
  ST16(ro, orow + q * 64 + (gg & 1u) * 32 + (gg & 2u) * 8, ov); \
  if (rg == q) { \
    s6 += (f32x4){bf_lo(r3[0]) + bf_lo(r4[0]) + bf_lo(r5[0]), bf_hi(r3[0]) + bf_hi(r4[0]) + bf_hi(r5[0]), bf_lo(r3[1]) + bf_lo(r4[1]) + bf_lo(r5[1]), bf_hi(r3[1]) + bf_hi(r4[1]) + bf_hi(r5[1])}; \
    ST8(ro, orow + 256 + gg * 8, fin4(s6, x6, gb2)); } \
} while (0)

#define SETUP() \
  const int T0 = rg * 2 + par, T1 = rg * 2 + (par ^ 1); \
  const int xr = 4 * g + q4; \
  u32 pX0_0 = lds0 + xr * 256 + (((2 * T0 + (p4 >> 1)) ^ ((xr & 7) << 1)) * 16) + (p4 & 1) * 8; \
  u32 pX1_0 = lds0 + xr * 256 + (((2 * T1 + (p4 >> 1)) ^ ((xr & 7) << 1)) * 16) + (p4 & 1) * 8; \
  u32 pW_0 = lds0 + l16; \
  u32 pV = lds0 + KC_MISC + 8192 + g * 16; \
  u32 pS = lds0 + KC_MISC + 12288 + wave * 128 + i16 * 8; \
  OPQ(pX0_0); OPQ(pX1_0); OPQ(pW_0); OPQ(pV); OPQ(pS); \
  u32 sHR0 = WS_HF + (u32)((rt * 8 + T0) * 144 + q * 36) * 1024; \
  u32 sHR1 = WS_HF + (u32)((rt * 8 + T1) * 144 + q * 36) * 1024; \
  u32 sHS0 = sHR0 + j * 9216, sHS1 = sHR1 + j * 9216; \
  u32 sFL = WS_FLAG + ((rt * 4 + q) * 4) * 64; \
  u32 sFQ = WS_FLAG + (rt * 16 + j) * 64; \
  u32 sPT = WS_PART + (u32)(rt * 4 + j) * (4 * 4 * 8 * 1024); \
  u32 sP8 = PSLOT(rg, 3, ((q - rg - 1) & 3) * 2 + par); \
  u32 sR8 = PSLOT(q, 3, par); \
  u32 sXR = WS_X1S + rt * 147456 + (j * 9 + 2 * q) * 4096 + rg * 2048; \
  u32 sXR8 = WS_X1S + rt * 147456 + (j * 9 + 8) * 4096 + rg * 1024; \
  u32 sGB = WS_GB + (j * 9 + 2 * q) * 64, sGB8 = WS_GB + (j * 9 + 8) * 64; \
  OPQS(sHR0); OPQS(sHR1); OPQS(sHS0); OPQS(sHS1); OPQS(sFL); OPQS(sFQ); OPQS(sPT); OPQS(sP8); OPQS(sR8); OPQS(sXR); OPQS(sXR8); OPQS(sGB); OPQS(sGB8); \
  u32 sHJ0 = j * 9216, sHJ1 = ((j + 1) & 3) * 9216, sHJ2 = ((j + 2) & 3) * 9216, sHJ3 = ((j + 3) & 3) * 9216; \
  OPQS(sHJ0); OPQS(sHJ1); OPQS(sHJ2); OPQS(sHJ3); \
  u32x4 ones4 = {0x3f803f80u, 0x3f803f80u, 0x3f803f80u, 0x3f803f80u}; OPQ(ones4); \
  float xs0_0, xs0_1, xs0_2, xs0_3, xs1_0, xs1_1, xs1_2, xs1_3, xs2_0, xs2_1, xs2_2, xs2_3, xs3_0, xs3_1, xs3_2, xs3_3; \
  float xs4_0, xs4_1, xs4_2, xs4_3, xs5_0, xs5_1, xs5_2, xs5_3, xs6_0, xs6_1, xs6_2, xs6_3;

extern "C" __global__ __launch_bounds__(512, 2) void kc_mlp(char* __restrict__ ws, u16* __restrict__ out) {
  extern __shared__ __attribute__((aligned(16))) char smem[];
  const int tid = threadIdx.x, lane = tid & 63;
  const int wave = __builtin_amdgcn_readfirstlane(tid >> 6);
  const int i16 = lane & 15, g = lane >> 4, q4 = i16 >> 2, p4 = i16 & 3;
  const int rg = wave & 3, par = wave >> 2;
  const int b = blockIdx.x, xcd = b & 7, idx = b >> 3;
  const int rt = (xcd & 1) * 8 + (idx >> 2), q = xcd >> 1, j = idx & 3, cj = q * 4 + j;
  const auto rs = MKRSRC(ws, WS_END);
  const u32x4 rs4 = {(u32)(uintptr_t)ws, (u32)((uintptr_t)ws >> 32) & 0xffffu, WS_END, 0x00020000u};
  const u32 l16 = lane * 16;
  const u32 xs = WS_X1S + rt * 147456 + rg * 2048;
  const u32 wsrc = WS_WST + cj * 648 * 1024 + rg * 2048;
  const u32 lds0 = (u32)(uintptr_t)(LAS char*)smem;
  const u32 lbase = lds0 + rg * 2048;
  {
    int c = wave * 64 + lane;
    c = c < 432 ? c : 431;
    const int seg = c / 72, o = c - seg * 72;
    const u32 vo = seg * (F_ * 4) + o * 16;
    DMA1(vo, WS_VP + cj * 288 * 4, lds0 + KC_MISC + wave * 1024);
  }
  if (par == 0) {
    DMA2(xs, 0, 0);
    DMA2(xs, 8192, 16384);
    DMA2(xs, 16384, 32768);
    DMA2(xs, 24576, 49152);
    SETUP();
    WAITV(4); BAR();
    VSUM();
    const u32x2 xl0_0 = TRR(pX0_0, 0), xh0_0 = TRR(pX0_0, 4096); const u32x4 X0_0 = {xl0_0[0], xl0_0[1], xh0_0[0], xh0_0[1]};
    const u32x2 xl1_0 = TRR(pX1_0, 0), xh1_0 = TRR(pX1_0, 4096); const u32x4 X1_0 = {xl1_0[0], xl1_0[1], xh1_0[0], xh1_0[1]};
    const u32x2 xl0_1 = TRR(pX0_0, 16384), xh0_1 = TRR(pX0_0, 20480); const u32x4 X0_1 = {xl0_1[0], xl0_1[1], xh0_1[0], xh0_1[1]};
    const u32x4 a0_0_0 = LDSR(pW_0, 8192);
    const u32x4 a0_0_1 = LDSR(pW_0, 9216);
    const u32x4 a1_0_0 = LDSR(pW_0, 10240);
    const u32x4 a1_0_1 = LDSR(pW_0, 11264);
    f32x4 c0_0_0; MFMA0(c0_0_0, a0_0_0, X0_0); SCHED();
    const u32x2 xl1_1 = TRR(pX1_0, 16384), xh1_1 = TRR(pX1_0, 20480); const u32x4 X1_1 = {xl1_1[0], xl1_1[1], xh1_1[0], xh1_1[1]};
    DMA2(xs, 32768, 65536);
    f32x4 c0_0_1; MFMA0(c0_0_1, a0_0_0, X1_0); SCHED();
    const u32x4 a0_1_0 = LDSR(pW_0, 24576);
    f32x4 c0_1_0; MFMA0(c0_1_0, a0_0_1, X0_0); SCHED();
    f32x4 c0_1_1; MFMA0(c0_1_1, a0_0_1, X1_0); SCHED();
    const u32x4 a0_1_1 = LDSR(pW_0, 25600);
    f32x4 c1_0_0; MFMA0(c1_0_0, a1_0_0, X0_0); SCHED();
    DMA2(xs, 40960, 81920);
    f32x4 c1_0_1; MFMA0(c1_0_1, a1_0_0, X1_0); SCHED();
    const u32x4 a1_1_0 = LDSR(pW_0, 26624);
    f32x4 c1_1_0; MFMA0(c1_1_0, a1_0_1, X0_0); SCHED();
    f32x4 c1_1_1; MFMA0(c1_1_1, a1_0_1, X1_0); SCHED();
    const u32x4 a1_1_1 = LDSR(pW_0, 27648);
    f32x4 stS; MFMA0(stS, ones4, X0_0); SCHED();
    f32x4 stQ; MFMA0(stQ, X0_0, X0_0); SCHED();
    WAITV(6); WAITL0(); BAR();
    const u32x2 xl0_2 = TRR(pX0_0, 32768), xh0_2 = TRR(pX0_0, 36864); const u32x4 X0_2 = {xl0_2[0], xl0_2[1], xh0_2[0], xh0_2[1]};
    MFMA(c0_0_0, a0_1_0, X0_1); SCHED();
    const u32x2 xl1_2 = TRR(pX1_0, 32768), xh1_2 = TRR(pX1_0, 36864); const u32x4 X1_2 = {xl1_2[0], xl1_2[1], xh1_2[0], xh1_2[1]};
    DMA2(xs, 49152, 98304);
    MFMA(c0_0_1, a0_1_0, X1_1); SCHED();
    const u32x4 a0_2_0 = LDSR(pW_0, 40960);
    MFMA(c0_1_0, a0_1_1, X0_1); SCHED();
    MFMA(c0_1_1, a0_1_1, X1_1); SCHED();
    const u32x4 a0_2_1 = LDSR(pW_0, 41984);
    MFMA(c1_0_0, a1_1_0, X0_1); SCHED();
    DMA2(xs, 57344, 114688);
    MFMA(c1_0_1, a1_1_0, X1_1); SCHED();
    const u32x4 a1_2_0 = LDSR(pW_0, 43008);
    MFMA(c1_1_0, a1_1_1, X0_1); SCHED();
    MFMA(c1_1_1, a1_1_1, X1_1); SCHED();
    const u32x4 a1_2_1 = LDSR(pW_0, 44032);
    MFMA(stS, ones4, X0_1); SCHED();
    MFMA(stQ, X0_1, X0_1); SCHED();
    WAITV(8); BAR();
    const u32x2 xl0_3 = TRR(pX0_0, 49152), xh0_3 = TRR(pX0_0, 53248); const u32x4 X0_3 = {xl0_3[0], xl0_3[1], xh0_3[0], xh0_3[1]};
    MFMA(c0_0_0, a0_2_0, X0_2); SCHED();
    const u32x2 xl1_3 = TRR(pX1_0, 49152), xh1_3 = TRR(pX1_0, 53248); const u32x4 X1_3 = {xl1_3[0], xl1_3[1], xh1_3[0], xh1_3[1]};
    DMA2(xs, 65536, 131072);
    MFMA(c0_0_1, a0_2_0, X1_2); SCHED();
    const u32x4 a0_3_0 = LDSR(pW_0, 57344);
    MFMA(c0_1_0, a0_2_1, X0_2); SCHED();
    MFMA(c0_1_1, a0_2_1, X1_2); SCHED();
    const u32x4 a0_3_1 = LDSR(pW_0, 58368);
    MFMA(c1_0_0, a1_2_0, X0_2); SCHED();
    MFMA(c1_0_1, a1_2_0, X1_2); SCHED();
    const u32x4 a1_3_0 = LDSR(pW_0, 59392);
    MFMA(c1_1_0, a1_2_1, X0_2); SCHED();
    MFMA(c1_1_1, a1_2_1, X1_2); SCHED();
    const u32x4 a1_3_1 = LDSR(pW_0, 60416);
    MFMA(stS, ones4, X0_2); SCHED();
    MFMA(stQ, X0_2, X0_2); SCHED();
    WAITV(8); BAR();
    WIN(pX0, 1);
    const u32x2 xl0_4 = TRR(pX0_1, 0), xh0_4 = TRR(pX0_1, 4096); const u32x4 X0_4 = {xl0_4[0], xl0_4[1], xh0_4[0], xh0_4[1]};
    MFMA(c0_0_0, a0_3_0, X0_3); SCHED();
    WIN(pX1, 1);
    const u32x2 xl1_4 = TRR(pX1_1, 0), xh1_4 = TRR(pX1_1, 4096); const u32x4 X1_4 = {xl1_4[0], xl1_4[1], xh1_4[0], xh1_4[1]};
    DMA2(xs, 73728, 0);
    MFMA(c0_0_1, a0_3_0, X1_3); SCHED();
    WIN(pW, 1);
    const u32x4 a0_4_0 = LDSR(pW_1, 8192);
    MFMA(c0_1_0, a0_3_1, X0_3); SCHED();
    MFMA(c0_1_1, a0_3_1, X1_3); SCHED();
    const u32x4 a0_4_1 = LDSR(pW_1, 9216);
    MFMA(c1_0_0, a1_3_0, X0_3); SCHED();
    MFMA(c1_0_1, a1_3_0, X1_3); SCHED();
    const u32x4 a1_4_0 = LDSR(pW_1, 10240);
    MFMA(c1_1_0, a1_3_1, X0_3); SCHED();
    MFMA(c1_1_1, a1_3_1, X1_3); SCHED();
    const u32x4 a1_4_1 = LDSR(pW_1, 11264);
    MFMA(stS, ones4, X0_3); SCHED();
    MFMA(stQ, X0_3, X0_3); SCHED();
    WAITV(8); BAR();
    const u32x2 xl0_5 = TRR(pX0_1, 16384), xh0_5 = TRR(pX0_1, 20480); const u32x4 X0_5 = {xl0_5[0], xl0_5[1], xh0_5[0], xh0_5[1]};
    MFMA(c0_0_0, a0_4_0, X0_4); SCHED();
    const u32x2 xl1_5 = TRR(pX1_1, 16384), xh1_5 = TRR(pX1_1, 20480); const u32x4 X1_5 = {xl1_5[0], xl1_5[1], xh1_5[0], xh1_5[1]};
    DMA2(xs, 81920, 16384);
    MFMA(c0_0_1, a0_4_0, X1_4); SCHED();
    const u32x4 a0_5_0 = LDSR(pW_1, 24576);
    MFMA(c0_1_0, a0_4_1, X0_4); SCHED();
    MFMA(c0_1_1, a0_4_1, X1_4); SCHED();
    const u32x4 a0_5_1 = LDSR(pW_1, 25600);
    MFMA(c1_0_0, a1_4_0, X0_4); SCHED();
    MFMA(c1_0_1, a1_4_0, X1_4); SCHED();
    const u32x4 a1_5_0 = LDSR(pW_1, 26624);
    MFMA(c1_1_0, a1_4_1, X0_4); SCHED();
    MFMA(c1_1_1, a1_4_1, X1_4); SCHED();
    const u32x4 a1_5_1 = LDSR(pW_1, 27648);
    MFMA(stS, ones4, X0_4); SCHED();
    MFMA(stQ, X0_4, X0_4); SCHED();
    WAITV(8); BAR();
    const u32x2 xl0_6 = TRR(pX0_1, 32768), xh0_6 = TRR(pX0_1, 36864); const u32x4 X0_6 = {xl0_6[0], xl0_6[1], xh0_6[0], xh0_6[1]};
    MFMA(c0_0_0, a0_5_0, X0_5); SCHED();
    const u32x2 xl1_6 = TRR(pX1_1, 32768), xh1_6 = TRR(pX1_1, 36864); const u32x4 X1_6 = {xl1_6[0], xl1_6[1], xh1_6[0], xh1_6[1]};
    DMA2(xs, 90112, 32768);
    MFMA(c0_0_1, a0_5_0, X1_5); SCHED();
    const u32x4 a0_6_0 = LDSR(pW_1, 40960);
    MFMA(c0_1_0, a0_5_1, X0_5); SCHED();
    MFMA(c0_1_1, a0_5_1, X1_5); SCHED();
    const u32x4 a0_6_1 = LDSR(pW_1, 41984);
    MFMA(c1_0_0, a1_5_0, X0_5); SCHED();
    MFMA(c1_0_1, a1_5_0, X1_5); SCHED();
    const u32x4 a1_6_0 = LDSR(pW_1, 43008);
    MFMA(c1_1_0, a1_5_1, X0_5); SCHED();
    MFMA(c1_1_1, a1_5_1, X1_5); SCHED();
    const u32x4 a1_6_1 = LDSR(pW_1, 44032);
    MFMA(stS, ones4, X0_5); SCHED();
    MFMA(stQ, X0_5, X0_5); SCHED();
    WAITV(8); BAR();
    const u32x2 xl0_7 = TRR(pX0_1, 49152), xh0_7 = TRR(pX0_1, 53248); const u32x4 X0_7 = {xl0_7[0], xl0_7[1], xh0_7[0], xh0_7[1]};
    MFMA(c0_0_0, a0_6_0, X0_6); SCHED();
    const u32x2 xl1_7 = TRR(pX1_1, 49152), xh1_7 = TRR(pX1_1, 53248); const u32x4 X1_7 = {xl1_7[0], xl1_7[1], xh1_7[0], xh1_7[1]};
    DMA2(xs, 98304, 49152);
    MFMA(c0_0_1, a0_6_0, X1_6); SCHED();
    const u32x4 a0_7_0 = LDSR(pW_1, 57344);
    MFMA(c0_1_0, a0_6_1, X0_6); SCHED();
    MFMA(c0_1_1, a0_6_1, X1_6); SCHED();
    const u32x4 a0_7_1 = LDSR(pW_1, 58368);
    MFMA(c1_0_0, a1_6_0, X0_6); SCHED();
    MFMA(c1_0_1, a1_6_0, X1_6); SCHED();
    const u32x4 a1_7_0 = LDSR(pW_1, 59392);
    MFMA(c1_1_0, a1_6_1, X0_6); SCHED();
    MFMA(c1_1_1, a1_6_1, X1_6); SCHED();
    const u32x4 a1_7_1 = LDSR(pW_1, 60416);
    MFMA(stS, ones4, X0_6); SCHED();
    MFMA(stQ, X0_6, X0_6); SCHED();
    WAITV(8); BAR();
    WIN(pX0, 2);
    const u32x2 xl0_8 = TRR(pX0_2, 0), xh0_8 = TRR(pX0_2, 4096); const u32x4 X0_8 = {xl0_8[0], xl0_8[1], xh0_8[0], xh0_8[1]};
    MFMA(c0_0_0, a0_7_0, X0_7); SCHED();
    WIN(pX1, 2);
    const u32x2 xl1_8 = TRR(pX1_2, 0), xh1_8 = TRR(pX1_2, 4096); const u32x4 X1_8 = {xl1_8[0], xl1_8[1], xh1_8[0], xh1_8[1]};
    DMA2(xs, 106496, 65536);
    MFMA(c0_0_1, a0_7_0, X1_7); SCHED();
    WIN(pW, 2);
    const u32x4 a0_8_0 = LDSR(pW_2, 8192);
    MFMA(c0_1_0, a0_7_1, X0_7); SCHED();
    MFMA(c0_1_1, a0_7_1, X1_7); SCHED();
    const u32x4 a0_8_1 = LDSR(pW_2, 9216);
    MFMA(c1_0_0, a1_7_0, X0_7); SCHED();
    MFMA(c1_0_1, a1_7_0, X1_7); SCHED();
    const u32x4 a1_8_0 = LDSR(pW_2, 10240);
    MFMA(c1_1_0, a1_7_1, X0_7); SCHED();
    MFMA(c1_1_1, a1_7_1, X1_7); SCHED();
    const u32x4 a1_8_1 = LDSR(pW_2, 11264);
    MFMA(stS, ones4, X0_7); SCHED();
    MFMA(stQ, X0_7, X0_7); SCHED();
    WAITV(8); BAR();
    const u32x2 xl0_9 = TRR(pX0_0, 0), xh0_9 = TRR(pX0_0, 4096); const u32x4 X0_9 = {xl0_9[0], xl0_9[1], xh0_9[0], xh0_9[1]};
    MFMA(c0_0_0, a0_8_0, X0_8); SCHED();
    const u32x2 xl1_9 = TRR(pX1_0, 0), xh1_9 = TRR(pX1_0, 4096); const u32x4 X1_9 = {xl1_9[0], xl1_9[1], xh1_9[0], xh1_9[1]};
    DMA2(xs, 114688, 81920);
    MFMA(c0_0_1, a0_8_0, X1_8); SCHED();
    const u32x4 a0_9_0 = LDSR(pW_0, 8192);
    MFMA(c0_1_0, a0_8_1, X0_8); SCHED();
    MFMA(c0_1_1, a0_8_1, X1_8); SCHED();
    const u32x4 a0_9_1 = LDSR(pW_0, 9216);
    MFMA(c1_0_0, a1_8_0, X0_8); SCHED();
    MFMA(c1_0_1, a1_8_0, X1_8); SCHED();
    const u32x4 a1_9_0 = LDSR(pW_0, 10240);
    MFMA(c1_1_0, a1_8_1, X0_8); SCHED();
    MFMA(c1_1_1, a1_8_1, X1_8); SCHED();
    const u32x4 a1_9_1 = LDSR(pW_0, 11264);
    MFMA(stS, ones4, X0_8); SCHED();
    MFMA(stQ, X0_8, X0_8); SCHED();
    WAITV(8); BAR();
    const u32x2 xl0_10 = TRR(pX0_0, 16384), xh0_10 = TRR(pX0_0, 20480); const u32x4 X0_10 = {xl0_10[0], xl0_10[1], xh0_10[0], xh0_10[1]};
    MFMA(c0_0_0, a0_9_0, X0_9); SCHED();
    const u32x2 xl1_10 = TRR(pX1_0, 16384), xh1_10 = TRR(pX1_0, 20480); const u32x4 X1_10 = {xl1_10[0], xl1_10[1], xh1_10[0], xh1_10[1]};
    DMA2(xs, 122880, 98304);
    MFMA(c0_0_1, a0_9_0, X1_9); SCHED();
    const u32x4 a0_10_0 = LDSR(pW_0, 24576);
    MFMA(c0_1_0, a0_9_1, X0_9); SCHED();
    MFMA(c0_1_1, a0_9_1, X1_9); SCHED();
    const u32x4 a0_10_1 = LDSR(pW_0, 25600);
    MFMA(c1_0_0, a1_9_0, X0_9); SCHED();
    MFMA(c1_0_1, a1_9_0, X1_9); SCHED();
    const u32x4 a1_10_0 = LDSR(pW_0, 26624);
    MFMA(c1_1_0, a1_9_1, X0_9); SCHED();
    MFMA(c1_1_1, a1_9_1, X1_9); SCHED();
    const u32x4 a1_10_1 = LDSR(pW_0, 27648);
    MFMA(stS, ones4, X0_9); SCHED();
    MFMA(stQ, X0_9, X0_9); SCHED();
    WAITV(8); BAR();
    const u32x2 xl0_11 = TRR(pX0_0, 32768), xh0_11 = TRR(pX0_0, 36864); const u32x4 X0_11 = {xl0_11[0], xl0_11[1], xh0_11[0], xh0_11[1]};
    MFMA(c0_0_0, a0_10_0, X0_10); SCHED();
    const u32x2 xl1_11 = TRR(pX1_0, 32768), xh1_11 = TRR(pX1_0, 36864); const u32x4 X1_11 = {xl1_11[0], xl1_11[1], xh1_11[0], xh1_11[1]};
    DMA2(xs, 131072, 114688);
    MFMA(c0_0_1, a0_10_0, X1_10); SCHED();
    const u32x4 a0_11_0 = LDSR(pW_0, 40960);
    MFMA(c0_1_0, a0_10_1, X0_10); SCHED();
    MFMA(c0_1_1, a0_10_1, X1_10); SCHED();
    const u32x4 a0_11_1 = LDSR(pW_0, 41984);
    MFMA(c1_0_0, a1_10_0, X0_10); SCHED();
    MFMA(c1_0_1, a1_10_0, X1_10); SCHED();
    const u32x4 a1_11_0 = LDSR(pW_0, 43008);
    MFMA(c1_1_0, a1_10_1, X0_10); SCHED();
    MFMA(c1_1_1, a1_10_1, X1_10); SCHED();
    const u32x4 a1_11_1 = LDSR(pW_0, 44032);
    MFMA(stS, ones4, X0_10); SCHED();
    MFMA(stQ, X0_10, X0_10); SCHED();
    WAITV(8); BAR();
    const u32x2 xl0_12 = TRR(pX0_0, 49152), xh0_12 = TRR(pX0_0, 53248); const u32x4 X0_12 = {xl0_12[0], xl0_12[1], xh0_12[0], xh0_12[1]};
    MFMA(c0_0_0, a0_11_0, X0_11); SCHED();
    const u32x2 xl1_12 = TRR(pX1_0, 49152), xh1_12 = TRR(pX1_0, 53248); const u32x4 X1_12 = {xl1_12[0], xl1_12[1], xh1_12[0], xh1_12[1]};
    DMA2(xs, 139264, 131072);
    MFMA(c0_0_1, a0_11_0, X1_11); SCHED();
    const u32x4 a0_12_0 = LDSR(pW_0, 57344);
    MFMA(c0_1_0, a0_11_1, X0_11); SCHED();
    MFMA(c0_1_1, a0_11_1, X1_11); SCHED();
    const u32x4 a0_12_1 = LDSR(pW_0, 58368);
    MFMA(c1_0_0, a1_11_0, X0_11); SCHED();
    MFMA(c1_0_1, a1_11_0, X1_11); SCHED();
    const u32x4 a1_12_0 = LDSR(pW_0, 59392);
    MFMA(c1_1_0, a1_11_1, X0_11); SCHED();
    MFMA(c1_1_1, a1_11_1, X1_11); SCHED();
    const u32x4 a1_12_1 = LDSR(pW_0, 60416);
    MFMA(stS, ones4, X0_11); SCHED();
    MFMA(stQ, X0_11, X0_11); SCHED();
    WAITV(8); BAR();
    const u32x2 xl0_13 = TRR(pX0_1, 0), xh0_13 = TRR(pX0_1, 4096); const u32x4 X0_13 = {xl0_13[0], xl0_13[1], xh0_13[0], xh0_13[1]};
    MFMA(c0_0_0, a0_12_0, X0_12); SCHED();
    const u32x2 xl1_13 = TRR(pX1_1, 0), xh1_13 = TRR(pX1_1, 4096); const u32x4 X1_13 = {xl1_13[0], xl1_13[1], xh1_13[0], xh1_13[1]};
    DMA2(wsrc, 147456, 0);
    MFMA(c0_0_1, a0_12_0, X1_12); SCHED();
    const u32x4 a0_13_0 = LDSR(pW_1, 8192);
    MFMA(c0_1_0, a0_12_1, X0_12); SCHED();
    MFMA(c0_1_1, a0_12_1, X1_12); SCHED();
    const u32x4 a0_13_1 = LDSR(pW_1, 9216);
    MFMA(c1_0_0, a1_12_0, X0_12); SCHED();
    MFMA(c1_0_1, a1_12_0, X1_12); SCHED();
    const u32x4 a1_13_0 = LDSR(pW_1, 10240);
    MFMA(c1_1_0, a1_12_1, X0_12); SCHED();
    MFMA(c1_1_1, a1_12_1, X1_12); SCHED();
    const u32x4 a1_13_1 = LDSR(pW_1, 11264);
    MFMA(stS, ones4, X0_12); SCHED();
    MFMA(stQ, X0_12, X0_12); SCHED();
    WAITV(8); BAR();
    const u32x2 xl0_14 = TRR(pX0_1, 16384), xh0_14 = TRR(pX0_1, 20480); const u32x4 X0_14 = {xl0_14[0], xl0_14[1], xh0_14[0], xh0_14[1]};
    MFMA(c0_0_0, a0_13_0, X0_13); SCHED();
    const u32x2 xl1_14 = TRR(pX1_1, 16384), xh1_14 = TRR(pX1_1, 20480); const u32x4 X1_14 = {xl1_14[0], xl1_14[1], xh1_14[0], xh1_14[1]};
    DMA2(wsrc, 163840, 16384);
    MFMA(c0_0_1, a0_13_0, X1_13); SCHED();
    const u32x4 a0_14_0 = LDSR(pW_1, 24576);
    MFMA(c0_1_0, a0_13_1, X0_13); SCHED();
    MFMA(c0_1_1, a0_13_1, X1_13); SCHED();
    const u32x4 a0_14_1 = LDSR(pW_1, 25600);
    MFMA(c1_0_0, a1_13_0, X0_13); SCHED();
    MFMA(c1_0_1, a1_13_0, X1_13); SCHED();
    const u32x4 a1_14_0 = LDSR(pW_1, 26624);
    MFMA(c1_1_0, a1_13_1, X0_13); SCHED();
    MFMA(c1_1_1, a1_13_1, X1_13); SCHED();
    const u32x4 a1_14_1 = LDSR(pW_1, 27648);
    MFMA(stS, ones4, X0_13); SCHED();
    MFMA(stQ, X0_13, X0_13); SCHED();
    WAITV(8); BAR();
    const u32x2 xl0_15 = TRR(pX0_1, 32768), xh0_15 = TRR(pX0_1, 36864); const u32x4 X0_15 = {xl0_15[0], xl0_15[1], xh0_15[0], xh0_15[1]};
    MFMA(c0_0_0, a0_14_0, X0_14); SCHED();
    const u32x2 xl1_15 = TRR(pX1_1, 32768), xh1_15 = TRR(pX1_1, 36864); const u32x4 X1_15 = {xl1_15[0], xl1_15[1], xh1_15[0], xh1_15[1]};
    DMA2(wsrc, 180224, 32768);
    MFMA(c0_0_1, a0_14_0, X1_14); SCHED();
    const u32x4 a0_15_0 = LDSR(pW_1, 40960);
    MFMA(c0_1_0, a0_14_1, X0_14); SCHED();
    MFMA(c0_1_1, a0_14_1, X1_14); SCHED();
    const u32x4 a0_15_1 = LDSR(pW_1, 41984);
    MFMA(c1_0_0, a1_14_0, X0_14); SCHED();
    MFMA(c1_0_1, a1_14_0, X1_14); SCHED();
    const u32x4 a1_15_0 = LDSR(pW_1, 43008);
    MFMA(c1_1_0, a1_14_1, X0_14); SCHED();
    MFMA(c1_1_1, a1_14_1, X1_14); SCHED();
    const u32x4 a1_15_1 = LDSR(pW_1, 44032);
    MFMA(stS, ones4, X0_14); SCHED();
    MFMA(stQ, X0_14, X0_14); SCHED();
    WAITV(8); BAR();
    const u32x2 xl0_16 = TRR(pX0_1, 49152), xh0_16 = TRR(pX0_1, 53248); const u32x4 X0_16 = {xl0_16[0], xl0_16[1], xh0_16[0], xh0_16[1]};
    MFMA(c0_0_0, a0_15_0, X0_15); SCHED();
    const u32x2 xl1_16 = TRR(pX1_1, 49152), xh1_16 = TRR(pX1_1, 53248); const u32x4 X1_16 = {xl1_16[0], xl1_16[1], xh1_16[0], xh1_16[1]};
    DMA2(wsrc, 196608, 49152);
    MFMA(c0_0_1, a0_15_0, X1_15); SCHED();
    const u32x4 a0_16_0 = LDSR(pW_1, 57344);
    MFMA(c0_1_0, a0_15_1, X0_15); SCHED();
    MFMA(c0_1_1, a0_15_1, X1_15); SCHED();
    const u32x4 a0_16_1 = LDSR(pW_1, 58368);
    MFMA(c1_0_0, a1_15_0, X0_15); SCHED();
    MFMA(c1_0_1, a1_15_0, X1_15); SCHED();
    const u32x4 a1_16_0 = LDSR(pW_1, 59392);
    MFMA(c1_1_0, a1_15_1, X0_15); SCHED();
    MFMA(c1_1_1, a1_15_1, X1_15); SCHED();
    const u32x4 a1_16_1 = LDSR(pW_1, 60416);
    MFMA(stS, ones4, X0_15); SCHED();
    MFMA(stQ, X0_15, X0_15); SCHED();
    WAITV(8); BAR();
    const u32x2 xl0_17 = TRR(pX0_2, 0), xh0_17 = TRR(pX0_2, 4096); const u32x4 X0_17 = {xl0_17[0], xl0_17[1], xh0_17[0], xh0_17[1]};
    MFMA(c0_0_0, a0_16_0, X0_16); SCHED();
    const u32x2 xl1_17 = TRR(pX1_2, 0), xh1_17 = TRR(pX1_2, 4096); const u32x4 X1_17 = {xl1_17[0], xl1_17[1], xh1_17[0], xh1_17[1]};
    DMA2(wsrc, 212992, 65536);
    MFMA(c0_0_1, a0_16_0, X1_16); SCHED();
    const u32x4 a0_17_0 = LDSR(pW_2, 8192);
    MFMA(c0_1_0, a0_16_1, X0_16); SCHED();
    MFMA(c0_1_1, a0_16_1, X1_16); SCHED();
    const u32x4 a0_17_1 = LDSR(pW_2, 9216);
    MFMA(c1_0_0, a1_16_0, X0_16); SCHED();
    MFMA(c1_0_1, a1_16_0, X1_16); SCHED();
    const u32x4 a1_17_0 = LDSR(pW_2, 10240);
    MFMA(c1_1_0, a1_16_1, X0_16); SCHED();
    MFMA(c1_1_1, a1_16_1, X1_16); SCHED();
    const u32x4 a1_17_1 = LDSR(pW_2, 11264);
    MFMA(stS, ones4, X0_16); SCHED();
    MFMA(stQ, X0_16, X0_16); SCHED();
    WAITV(8); BAR();
    MFMA(c0_0_0, a0_17_0, X0_17); SCHED();
    DMA2(wsrc, 229376, 81920);
    MFMA(c0_0_1, a0_17_0, X1_17); SCHED();
    const u32x4 a2_0_0 = LDSR(pW_0, 0);
    MFMA(c0_1_0, a0_17_1, X0_17); SCHED();
    MFMA(c0_1_1, a0_17_1, X1_17); SCHED();
    const u32x4 a2_0_1 = LDSR(pW_0, 1024);
    MFMA(c1_0_0, a1_17_0, X0_17); SCHED();
    MFMA(c1_0_1, a1_17_0, X1_17); SCHED();
    const u32x4 a2_1_0 = LDSR(pW_0, 4096);
    MFMA(c1_1_0, a1_17_1, X0_17); SCHED();
    MFMA(c1_1_1, a1_17_1, X1_17); SCHED();
    const u32x4 a2_1_1 = LDSR(pW_0, 5120);
    MFMA(stS, ones4, X0_17); SCHED();
    MFMA(stQ, X0_17, X0_17); SCHED();
    WAITV(8); BAR();
    const u32x4 a2_2_0 = LDSR(pW_0, 8192);
    f32x4 c2_0_0; MFMA0(c2_0_0, a2_0_0, X0_0); SCHED();
    f32x4 c2_0_1; MFMA0(c2_0_1, a2_0_0, X1_0); SCHED();
    const u32x4 a2_2_1 = LDSR(pW_0, 9216);
    f32x4 c2_1_0; MFMA0(c2_1_0, a2_0_1, X0_0); SCHED();
    f32x4 c2_1_1; MFMA0(c2_1_1, a2_0_1, X1_0); SCHED();
    const u32x4 a2_3_0 = LDSR(pW_0, 12288);
    float qd_; STAT_DIAG(qd_); asm volatile("" :: "v"(stS));
    MFMA(c2_0_0, a2_1_0, X0_1); SCHED();
    STAT_BCAST(qd_);
    MFMA(c2_0_1, a2_1_0, X1_1); SCHED();
    const u32x4 a2_3_1 = LDSR(pW_0, 13312);
    float mean_; V_MULC(mean_, stS[0], 1.f / D_);
    PREFILL(0);
    MFMA(c2_1_0, a2_1_1, X0_1); SCHED();
    float m2_ = mean_ * mean_; OPQ(m2_); mean_ = rbf(mean_); OPQ(mean_);
    DMA2(wsrc, 245760, 98304);
    MFMA(c2_1_1, a2_1_1, X1_1); SCHED();
    const u32x4 a2_4_0 = LDSR(pW_0, 16384);
    MFMA(c2_0_0, a2_2_0, X0_2); SCHED();
    float var_ = fmaxf(__builtin_fmaf(qd_, 1.f / D_, -m2_), 0.f); OPQ(var_);
    MFMA(c2_0_1, a2_2_0, X1_2); SCHED();
    const u32x4 a2_4_1 = LDSR(pW_0, 17408);
    var_ = rbf(var_) + 1e-5f; OPQ(var_);
    MFMA(c2_1_0, a2_2_1, X0_2); SCHED();
    const f32x4 v1_r0_0_0 = LDSRF(pV, 0), v2_r0_0_0 = LDSRF(pV, 1152);
    var_ = __builtin_amdgcn_sqrtf(rbf(var_)); OPQ(var_);
    MFMA(c2_1_1, a2_2_1, X1_2); SCHED();
    const u32x4 a2_5_0 = LDSR(pW_0, 20480);
    var_ = __builtin_amdgcn_rcpf(rbf(var_)); OPQ(var_);
    MFMA(c2_0_0, a2_3_0, X0_3); SCHED();
    float R0 = var_ * (-LOG2E); OPQ(R0); float M0 = -(mean_ * R0); OPQ(M0);
    MFMA(c2_0_1, a2_3_0, X1_3); SCHED();
    const u32x4 a2_5_1 = LDSR(pW_0, 21504);
    { const f32x2 mr_ = {M0, R0}; LDSW2(pS, 0, mr_); }
    MFMA(c2_1_0, a2_3_1, X0_3); SCHED();
    float t0a; V_FMA(t0a, M0, v1_r0_0_0[0], v2_r0_0_0[0]);
    float t0b; V_FMA(t0b, M0, v1_r0_0_0[1], v2_r0_0_0[1]);
    PREFILL(1);
    MFMA(c2_1_1, a2_3_1, X1_3); SCHED();
    const u32x4 a2_6_0 = LDSR(pW_0, 24576);
    V_FMAC(t0a, c0_0_0[0], R0);
    V_FMAC(t0b, c0_0_0[1], R0);
    MFMA(c2_0_0, a2_4_0, X0_4); SCHED();
    float e0a; V_EXP(e0a, t0a);
    float e0b; V_EXP(e0b, t0b);
    MFMA(c2_0_1, a2_4_0, X1_4); SCHED();
    const u32x4 a2_6_1 = LDSR(pW_0, 25600);
    V_ADD1(e0a);
    V_ADD1(e0b);
    DMA2(wsrc, 262144, 114688);
    MFMA(c2_1_0, a2_4_1, X0_4); SCHED();
    V_RCP(e0a);
    V_RCP(e0b);
    MFMA(c2_1_1, a2_4_1, X1_4); SCHED();
    const u32x4 a2_7_0 = LDSR(pW_0, 28672);
    V_MUL(t0a, e0a);
    V_MUL(t0b, e0b);
    MFMA(c2_0_0, a2_5_0, X0_5); SCHED();
    u32 hp_r0_0_0; V_CVT(hp_r0_0_0, t0a, t0b);
    float t1a; V_FMA(t1a, M0, v1_r0_0_0[2], v2_r0_0_0[2]);
    MFMA(c2_0_1, a2_5_0, X1_5); SCHED();
    const f32x4 v1_r0_0_1 = LDSRF(pV, 64), v2_r0_0_1 = LDSRF(pV, 1216);
    const u32x4 a2_7_1 = LDSR(pW_0, 29696);
    float t1b; V_FMA(t1b, M0, v1_r0_0_0[3], v2_r0_0_0[3]);
    V_FMAC(t1a, c0_0_0[2], R0);
    MFMA(c2_1_0, a2_5_1, X0_5); SCHED();
    V_FMAC(t1b, c0_0_0[3], R0);
    float e1a; V_EXP(e1a, t1a);
    MFMA(c2_1_1, a2_5_1, X1_5); SCHED();
    WAITV(10); WAITL0(); BAR();
    const f32x2 mr1 = LDSR2(pS, 512); const float M1 = mr1[0], R1 = mr1[1];
    const u32x4 a2_8_0 = LDSR(pW_0, 32768);
    float e1b; V_EXP(e1b, t1b);
    V_ADD1(e1a);
    PREFILL(2);
    MFMA(c2_0_0, a2_6_0, X0_6); SCHED();
    V_ADD1(e1b);
    V_RCP(e1a);
    MFMA(c2_0_1, a2_6_0, X1_6); SCHED();
    const u32x4 a2_8_1 = LDSR(pW_0, 33792);
    V_RCP(e1b);
    V_MUL(t1a, e1a);
    MFMA(c2_1_0, a2_6_1, X0_6); SCHED();
    V_MUL(t1b, e1b);
    u32 hp_r0_0_1; V_CVT(hp_r0_0_1, t1a, t1b);
    MFMA(c2_1_1, a2_6_1, X1_6); SCHED();
    const u32x4 a2_9_0 = LDSR(pW_0, 36864);
    float t2a; V_FMA(t2a, M0, v1_r0_0_1[0], v2_r0_0_1[0]);
    MFMA(c2_0_0, a2_7_0, X0_7); SCHED();
    float t2b; V_FMA(t2b, M0, v1_r0_0_1[1], v2_r0_0_1[1]);
    V_FMAC(t2a, c0_1_0[0], R0);
    MFMA(c2_0_1, a2_7_0, X1_7); SCHED();
    const u32x4 a2_9_1 = LDSR(pW_0, 37888);
    V_FMAC(t2b, c0_1_0[1], R0);
    float e2a; V_EXP(e2a, t2a);
    MFMA(c2_1_0, a2_7_1, X0_7); SCHED();
    float e2b; V_EXP(e2b, t2b);
    V_ADD1(e2a);
    DMA2(wsrc, 278528, 131072);
    MFMA(c2_1_1, a2_7_1, X1_7); SCHED();
    const u32x4 a2_10_0 = LDSR(pW_0, 40960);
    V_ADD1(e2b);
    V_RCP(e2a);
    MFMA(c2_0_0, a2_8_0, X0_8); SCHED();
    V_RCP(e2b);
    V_MUL(t2a, e2a);
    PREFILL(3);
    MFMA(c2_0_1, a2_8_0, X1_8); SCHED();
    const u32x4 a2_10_1 = LDSR(pW_0, 41984);
    V_MUL(t2b, e2b);
    u32 hp_r0_0_2; V_CVT(hp_r0_0_2, t2a, t2b);
    MFMA(c2_1_0, a2_8_1, X0_8); SCHED();
    float t3a; V_FMA(t3a, M0, v1_r0_0_1[2], v2_r0_0_1[2]);
    float t3b; V_FMA(t3b, M0, v1_r0_0_1[3], v2_r0_0_1[3]);
    MFMA(c2_1_1, a2_8_1, X1_8); SCHED();
    const f32x4 v1_r0_1_0 = LDSRF(pV, 0), v2_r0_1_0 = LDSRF(pV, 1152);
    const u32x4 a2_11_0 = LDSR(pW_0, 45056);
    V_FMAC(t3a, c0_1_0[2], R0);
    V_FMAC(t3b, c0_1_0[3], R0);
    MFMA(c2_0_0, a2_9_0, X0_9); SCHED();
    float e3a; V_EXP(e3a, t3a);
    float e3b; V_EXP(e3b, t3b);
    MFMA(c2_0_1, a2_9_0, X1_9); SCHED();
    const u32x4 a2_11_1 = LDSR(pW_0, 46080);
    V_ADD1(e3a);
    V_ADD1(e3b);
    MFMA(c2_1_0, a2_9_1, X0_9); SCHED();
    V_RCP(e3a);
    V_RCP(e3b);
    MFMA(c2_1_1, a2_9_1, X1_9); SCHED();
    const u32x4 a2_12_0 = LDSR(pW_0, 49152);
    V_MUL(t3a, e3a);
    V_MUL(t3b, e3b);
    MFMA(c2_0_0, a2_10_0, X0_10); SCHED();
    u32 hp_r0_0_3; V_CVT(hp_r0_0_3, t3a, t3b);
    MFMA(c2_0_1, a2_10_0, X1_10); SCHED();
    const u32x4 a2_12_1 = LDSR(pW_0, 50176);
    float t4a; V_FMA(t4a, M1, v1_r0_1_0[0], v2_r0_1_0[0]);
    float t4b; V_FMA(t4b, M1, v1_r0_1_0[1], v2_r0_1_0[1]);
    DMA2(wsrc, 294912, 0);
    const u32x4 hv_r0_0 = {hp_r0_0_0, hp_r0_0_1, hp_r0_0_2, hp_r0_0_3}; HST(sHS0 + 0, hv_r0_0);
    MFMA(c2_1_0, a2_10_1, X0_10); SCHED();
    V_FMAC(t4a, c0_0_1[0], R1);
    V_FMAC(t4b, c0_0_1[1], R1);
    MFMA(c2_1_1, a2_10_1, X1_10); SCHED();
    const u32x4 a2_13_0 = LDSR(pW_0, 53248);
    float e4a; V_EXP(e4a, t4a);
    float e4b; V_EXP(e4b, t4b);
    MFMA(c2_0_0, a2_11_0, X0_11); SCHED();
    V_ADD1(e4a);
    V_ADD1(e4b);
    MFMA(c2_0_1, a2_11_0, X1_11); SCHED();
    const u32x4 a2_13_1 = LDSR(pW_0, 54272);
    V_RCP(e4a);
    V_RCP(e4b);
    MFMA(c2_1_0, a2_11_1, X0_11); SCHED();
    V_MUL(t4a, e4a);
    V_MUL(t4b, e4b);
    MFMA(c2_1_1, a2_11_1, X1_11); SCHED();
    WAITV(15); BAR();
    const u32x4 a2_14_0 = LDSR(pW_0, 57344);
    u32 hp_r0_1_0; V_CVT(hp_r0_1_0, t4a, t4b);
    float t5a; V_FMA(t5a, M1, v1_r0_1_0[2], v2_r0_1_0[2]);
    MFMA(c2_0_0, a2_12_0, X0_12); SCHED();
    const f32x4 v1_r0_1_1 = LDSRF(pV, 64), v2_r0_1_1 = LDSRF(pV, 1216);
    float t5b; V_FMA(t5b, M1, v1_r0_1_0[3], v2_r0_1_0[3]);
    V_FMAC(t5a, c0_0_1[2], R1);
    MFMA(c2_0_1, a2_12_0, X1_12); SCHED();
    const u32x4 a2_14_1 = LDSR(pW_0, 58368);
    V_FMAC(t5b, c0_0_1[3], R1);
    float e5a; V_EXP(e5a, t5a);
    MFMA(c2_1_0, a2_12_1, X0_12); SCHED();
    float e5b; V_EXP(e5b, t5b);
    V_ADD1(e5a);
    MFMA(c2_1_1, a2_12_1, X1_12); SCHED();
    const u32x4 a2_15_0 = LDSR(pW_0, 61440);
    V_ADD1(e5b);
    V_RCP(e5a);
    MFMA(c2_0_0, a2_13_0, X0_13); SCHED();
    V_RCP(e5b);
    V_MUL(t5a, e5a);
    MFMA(c2_0_1, a2_13_0, X1_13); SCHED();
    const u32x4 a2_15_1 = LDSR(pW_0, 62464);
    V_MUL(t5b, e5b);
    u32 hp_r0_1_1; V_CVT(hp_r0_1_1, t5a, t5b);
    MFMA(c2_1_0, a2_13_1, X0_13); SCHED();
    float t6a; V_FMA(t6a, M1, v1_r0_1_1[0], v2_r0_1_1[0]);
    DMA2(wsrc, 311296, 16384);
    MFMA(c2_1_1, a2_13_1, X1_13); SCHED();
    const u32x4 a2_16_0 = LDSR(pW_1, 0);
    float t6b; V_FMA(t6b, M1, v1_r0_1_1[1], v2_r0_1_1[1]);
    V_FMAC(t6a, c0_1_1[0], R1);
    MFMA(c2_0_0, a2_14_0, X0_14); SCHED();
    V_FMAC(t6b, c0_1_1[1], R1);
    float e6a; V_EXP(e6a, t6a);
    MFMA(c2_0_1, a2_14_0, X1_14); SCHED();
    const u32x4 a2_16_1 = LDSR(pW_1, 1024);
    float e6b; V_EXP(e6b, t6b);
    V_ADD1(e6a);
    MFMA(c2_1_0, a2_14_1, X0_14); SCHED();
    V_ADD1(e6b);
    V_RCP(e6a);
    MFMA(c2_1_1, a2_14_1, X1_14); SCHED();
    const u32x4 a2_17_0 = LDSR(pW_1, 4096);
    V_RCP(e6b);
    V_MUL(t6a, e6a);
    MFMA(c2_0_0, a2_15_0, X0_15); SCHED();
    V_MUL(t6b, e6b);
    u32 hp_r0_1_2; V_CVT(hp_r0_1_2, t6a, t6b);
    MFMA(c2_0_1, a2_15_0, X1_15); SCHED();
    const u32x4 a2_17_1 = LDSR(pW_1, 5120);
    float t7a; V_FMA(t7a, M1, v1_r0_1_1[2], v2_r0_1_1[2]);
    float t7b; V_FMA(t7b, M1, v1_r0_1_1[3], v2_r0_1_1[3]);
    MFMA(c2_1_0, a2_15_1, X0_15); SCHED();
    V_FMAC(t7a, c0_1_1[2], R1);
    V_FMAC(t7b, c0_1_1[3], R1);
    MFMA(c2_1_1, a2_15_1, X1_15); SCHED();
    const u32x4 a3_0_0 = LDSR(pW_1, 8192);
    float e7a; V_EXP(e7a, t7a);
    float e7b; V_EXP(e7b, t7b);
    MFMA(c2_0_0, a2_16_0, X0_16); SCHED();
    V_ADD1(e7a);
    V_ADD1(e7b);
    MFMA(c2_0_1, a2_16_0, X1_16); SCHED();
    const u32x4 a3_0_1 = LDSR(pW_1, 9216);
    V_RCP(e7a);
    V_RCP(e7b);
    DMA2(wsrc, 327680, 32768);
    MFMA(c2_1_0, a2_16_1, X0_16); SCHED();
    const f32x4 v1_r1_0_0 = LDSRF(pV, 256), v2_r1_0_0 = LDSRF(pV, 1408);
    V_MUL(t7a, e7a);
    V_MUL(t7b, e7b);
    MFMA(c2_1_1, a2_16_1, X1_16); SCHED();
    const u32x4 a3_1_0 = LDSR(pW_1, 12288);
    u32 hp_r0_1_3; V_CVT(hp_r0_1_3, t7a, t7b);
    MFMA(c2_0_0, a2_17_0, X0_17); SCHED();
    const u32x4 hv_r0_1 = {hp_r0_1_0, hp_r0_1_1, hp_r0_1_2, hp_r0_1_3}; HST(sHS1 + 0, hv_r0_1);
    MFMA(c2_0_1, a2_17_0, X1_17); SCHED();
    const u32x4 a3_1_1 = LDSR(pW_1, 13312);
    MFMA(c2_1_0, a2_17_1, X0_17); SCHED();
    float t8a; V_FMA(t8a, M0, v1_r1_0_0[0], v2_r1_0_0[0]);
    float t8b; V_FMA(t8b, M0, v1_r1_0_0[1], v2_r1_0_0[1]);
    MFMA(c2_1_1, a2_17_1, X1_17); SCHED();
    WAITV(15); BAR();
    const u32x4 a3_2_0 = LDSR(pW_1, 16384);
    V_FMAC(t8a, c1_0_0[0], R0);
    V_FMAC(t8b, c1_0_0[1], R0);
    f32x4 c3_0_0; MFMA0(c3_0_0, a3_0_0, X0_0); SCHED();
    float e8a; V_EXP(e8a, t8a);
    float e8b; V_EXP(e8b, t8b);
    f32x4 c3_0_1; MFMA0(c3_0_1, a3_0_0, X1_0); SCHED();
    const u32x4 a3_2_1 = LDSR(pW_1, 17408);
    V_ADD1(e8a);
    V_ADD1(e8b);
    f32x4 c3_1_0; MFMA0(c3_1_0, a3_0_1, X0_0); SCHED();
    V_RCP(e8a);
    V_RCP(e8b);
    f32x4 c3_1_1; MFMA0(c3_1_1, a3_0_1, X1_0); SCHED();
    const u32x4 a3_3_0 = LDSR(pW_1, 20480);
    V_MUL(t8a, e8a);
    MFMA(c3_0_0, a3_1_0, X0_1); SCHED();
    V_MUL(t8b, e8b);
    u32 hp_r1_0_0; V_CVT(hp_r1_0_0, t8a, t8b);
    MFMA(c3_0_1, a3_1_0, X1_1); SCHED();
    const u32x4 a3_3_1 = LDSR(pW_1, 21504);
    float t9a; V_FMA(t9a, M0, v1_r1_0_0[2], v2_r1_0_0[2]);
    float t9b; V_FMA(t9b, M0, v1_r1_0_0[3], v2_r1_0_0[3]);
    MFMA(c3_1_0, a3_1_1, X0_1); SCHED();
    const f32x4 v1_r1_0_1 = LDSRF(pV, 320), v2_r1_0_1 = LDSRF(pV, 1472);
    V_FMAC(t9a, c1_0_0[2], R0);
    V_FMAC(t9b, c1_0_0[3], R0);
    DMA2(wsrc, 344064, 49152);
    MFMA(c3_1_1, a3_1_1, X1_1); SCHED();
    const u32x4 a3_4_0 = LDSR(pW_1, 24576);
    float e9a; V_EXP(e9a, t9a);
    float e9b; V_EXP(e9b, t9b);
    MFMA(c3_0_0, a3_2_0, X0_2); SCHED();
    V_ADD1(e9a);
    V_ADD1(e9b);
    MFMA(c3_0_1, a3_2_0, X1_2); SCHED();
    const u32x4 a3_4_1 = LDSR(pW_1, 25600);
    V_RCP(e9a);
    MFMA(c3_1_0, a3_2_1, X0_2); SCHED();
    V_RCP(e9b);
    V_MUL(t9a, e9a);
    MFMA(c3_1_1, a3_2_1, X1_2); SCHED();
    const u32x4 a3_5_0 = LDSR(pW_1, 28672);
    V_MUL(t9b, e9b);
    u32 hp_r1_0_1; V_CVT(hp_r1_0_1, t9a, t9b);
    MFMA(c3_0_0, a3_3_0, X0_3); SCHED();
    float t10a; V_FMA(t10a, M0, v1_r1_0_1[0], v2_r1_0_1[0]);
    float t10b; V_FMA(t10b, M0, v1_r1_0_1[1], v2_r1_0_1[1]);
    MFMA(c3_0_1, a3_3_0, X1_3); SCHED();
    const u32x4 a3_5_1 = LDSR(pW_1, 29696);
    V_FMAC(t10a, c1_1_0[0], R0);
    V_FMAC(t10b, c1_1_0[1], R0);
    MFMA(c3_1_0, a3_3_1, X0_3); SCHED();
    float e10a; V_EXP(e10a, t10a);
    float e10b; V_EXP(e10b, t10b);
    MFMA(c3_1_1, a3_3_1, X1_3); SCHED();
    const u32x4 a3_6_0 = LDSR(pW_1, 32768);
    V_ADD1(e10a);
    MFMA(c3_0_0, a3_4_0, X0_4); SCHED();
    V_ADD1(e10b);
    V_RCP(e10a);
    MFMA(c3_0_1, a3_4_0, X1_4); SCHED();
    const u32x4 a3_6_1 = LDSR(pW_1, 33792);
    V_RCP(e10b);
    V_MUL(t10a, e10a);
    DMA2(wsrc, 360448, 65536);
    MFMA(c3_1_0, a3_4_1, X0_4); SCHED();
    V_MUL(t10b, e10b);
    u32 hp_r1_0_2; V_CVT(hp_r1_0_2, t10a, t10b);
    MFMA(c3_1_1, a3_4_1, X1_4); SCHED();
    const u32x4 a3_7_0 = LDSR(pW_1, 36864);
    float t11a; V_FMA(t11a, M0, v1_r1_0_1[2], v2_r1_0_1[2]);
    float t11b; V_FMA(t11b, M0, v1_r1_0_1[3], v2_r1_0_1[3]);
    MFMA(c3_0_0, a3_5_0, X0_5); SCHED();
    const f32x4 v1_r1_1_0 = LDSRF(pV, 256), v2_r1_1_0 = LDSRF(pV, 1408);
    V_FMAC(t11a, c1_1_0[2], R0);
    MFMA(c3_0_1, a3_5_0, X1_5); SCHED();
    const u32x4 a3_7_1 = LDSR(pW_1, 37888);
    V_FMAC(t11b, c1_1_0[3], R0);
    float e11a; V_EXP(e11a, t11a);
    MFMA(c3_1_0, a3_5_1, X0_5); SCHED();
    float e11b; V_EXP(e11b, t11b);
    V_ADD1(e11a);
    MFMA(c3_1_1, a3_5_1, X1_5); SCHED();
    WAITV(19); BAR();
    const u32x4 a3_8_0 = LDSR(pW_1, 40960);
    V_ADD1(e11b);
    V_RCP(e11a);
    MFMA(c3_0_0, a3_6_0, X0_6); SCHED();
    V_RCP(e11b);
    V_MUL(t11a, e11a);
    MFMA(c3_0_1, a3_6_0, X1_6); SCHED();
    const u32x4 a3_8_1 = LDSR(pW_1, 41984);
    V_MUL(t11b, e11b);
    u32 hp_r1_0_3; V_CVT(hp_r1_0_3, t11a, t11b);
    MFMA(c3_1_0, a3_6_1, X0_6); SCHED();
    float t12a; V_FMA(t12a, M1, v1_r1_1_0[0], v2_r1_1_0[0]);
    DMA2(wsrc, 376832, 81920);
    const u32x4 hv_r1_0 = {hp_r1_0_0, hp_r1_0_1, hp_r1_0_2, hp_r1_0_3}; HST(sHS0 + 2048, hv_r1_0);
    MFMA(c3_1_1, a3_6_1, X1_6); SCHED();
    const u32x4 a3_9_0 = LDSR(pW_1, 45056);
    float t12b; V_FMA(t12b, M1, v1_r1_1_0[1], v2_r1_1_0[1]);
    V_FMAC(t12a, c1_0_1[0], R1);
    MFMA(c3_0_0, a3_7_0, X0_7); SCHED();
    V_FMAC(t12b, c1_0_1[1], R1);
    float e12a; V_EXP(e12a, t12a);
    MFMA(c3_0_1, a3_7_0, X1_7); SCHED();
    WAITV(7); BAR();
    const u32x4 a3_9_1 = LDSR(pW_1, 46080);
    float e12b; V_EXP(e12b, t12b);
    V_ADD1(e12a);
    MFMA(c3_1_0, a3_7_1, X0_7); SCHED();
    if (rg == 0) { if (l16 == 0) __builtin_amdgcn_raw_buffer_store_b32(1u, rs, 0, (int)(sFL + j * 64), 16); }
    V_ADD1(e12b);
    V_RCP(e12a);
    MFMA(c3_1_1, a3_7_1, X1_7); SCHED();
    const u32x4 a3_10_0 = LDSR(pW_1, 49152);
    V_RCP(e12b);
    V_MUL(t12a, e12a);
    MFMA(c3_0_0, a3_8_0, X0_8); SCHED();
    V_MUL(t12b, e12b);
    MFMA(c3_0_1, a3_8_0, X1_8); SCHED();
    const u32x4 a3_10_1 = LDSR(pW_1, 50176);
    u32 hp_r1_1_0; V_CVT(hp_r1_1_0, t12a, t12b);
    float t13a; V_FMA(t13a, M1, v1_r1_1_0[2], v2_r1_1_0[2]);
    MFMA(c3_1_0, a3_8_1, X0_8); SCHED();
    const f32x4 v1_r1_1_1 = LDSRF(pV, 320), v2_r1_1_1 = LDSRF(pV, 1472);
    float t13b; V_FMA(t13b, M1, v1_r1_1_0[3], v2_r1_1_0[3]);
    V_FMAC(t13a, c1_0_1[2], R1);
    MFMA(c3_1_1, a3_8_1, X1_8); SCHED();
    const u32x4 a3_11_0 = LDSR(pW_1, 53248);
    V_FMAC(t13b, c1_0_1[3], R1);
    float e13a; V_EXP(e13a, t13a);
    MFMA(c3_0_0, a3_9_0, X0_9); SCHED();
    float e13b; V_EXP(e13b, t13b);
    V_ADD1(e13a);
    MFMA(c3_0_1, a3_9_0, X1_9); SCHED();
    const u32x4 a3_11_1 = LDSR(pW_1, 54272);
    V_ADD1(e13b);
    MFMA(c3_1_0, a3_9_1, X0_9); SCHED();
    V_RCP(e13a);
    V_RCP(e13b);
    MFMA(c3_1_1, a3_9_1, X1_9); SCHED();
    const u32x4 a3_12_0 = LDSR(pW_1, 57344);
    V_MUL(t13a, e13a);
    V_MUL(t13b, e13b);
    MFMA(c3_0_0, a3_10_0, X0_10); SCHED();
    u32 hp_r1_1_1; V_CVT(hp_r1_1_1, t13a, t13b);
    float t14a; V_FMA(t14a, M1, v1_r1_1_1[0], v2_r1_1_1[0]);
    MFMA(c3_0_1, a3_10_0, X1_10); SCHED();
    const u32x4 a3_12_1 = LDSR(pW_1, 58368);
    float t14b; V_FMA(t14b, M1, v1_r1_1_1[1], v2_r1_1_1[1]);
    V_FMAC(t14a, c1_1_1[0], R1);
    MFMA(c3_1_0, a3_10_1, X0_10); SCHED();
    V_FMAC(t14b, c1_1_1[1], R1);
    float e14a; V_EXP(e14a, t14a);
    MFMA(c3_1_1, a3_10_1, X1_10); SCHED();
    const u32x4 a3_13_0 = LDSR(pW_1, 61440);
    float e14b; V_EXP(e14b, t14b);
    MFMA(c3_0_0, a3_11_0, X0_11); SCHED();
    V_ADD1(e14a);
    V_ADD1(e14b);
    MFMA(c3_0_1, a3_11_0, X1_11); SCHED();
    const u32x4 a3_13_1 = LDSR(pW_1, 62464);
    V_RCP(e14a);
    V_RCP(e14b);
    MFMA(c3_1_0, a3_11_1, X0_11); SCHED();
    V_MUL(t14a, e14a);
    V_MUL(t14b, e14b);
    MFMA(c3_1_1, a3_11_1, X1_11); SCHED();
    WAITV(13); BAR();
    const u32x4 a3_14_0 = LDSR(pW_2, 0);
    u32 hp_r1_1_2; V_CVT(hp_r1_1_2, t14a, t14b);
    float t15a; V_FMA(t15a, M1, v1_r1_1_1[2], v2_r1_1_1[2]);
    MFMA(c3_0_0, a3_12_0, X0_12); SCHED();
    float t15b; V_FMA(t15b, M1, v1_r1_1_1[3], v2_r1_1_1[3]);
    V_FMAC(t15a, c1_1_1[2], R1);
    MFMA(c3_0_1, a3_12_0, X1_12); SCHED();
    const u32x4 a3_14_1 = LDSR(pW_2, 1024);
    V_FMAC(t15b, c1_1_1[3], R1);
    DMA2(wsrc, 393216, 98304);
    MFMA(c3_1_0, a3_12_1, X0_12); SCHED();
    float e15a; V_EXP(e15a, t15a);
    float e15b; V_EXP(e15b, t15b);
    MFMA(c3_1_1, a3_12_1, X1_12); SCHED();
    const u32x4 a3_15_0 = LDSR(pW_2, 4096);
    V_ADD1(e15a);
    V_ADD1(e15b);
    u32 fl1; POLL(fl1, sFL, 2);
    MFMA(c3_0_0, a3_13_0, X0_13); SCHED();
    V_RCP(e15a);
    V_RCP(e15b);
    MFMA(c3_0_1, a3_13_0, X1_13); SCHED();
    const f32x4 v1_r2_0_0 = LDSRF(pV, 512), v2_r2_0_0 = LDSRF(pV, 1664);
    const u32x4 a3_15_1 = LDSR(pW_2, 5120);
    V_MUL(t15a, e15a);
    V_MUL(t15b, e15b);
    MFMA(c3_1_0, a3_13_1, X0_13); SCHED();
    u32 hp_r1_1_3; V_CVT(hp_r1_1_3, t15a, t15b);
    MFMA(c3_1_1, a3_13_1, X1_13); SCHED();
    const u32x4 a3_16_0 = LDSR(pW_2, 8192);
    const u32x4 hv_r1_1 = {hp_r1_1_0, hp_r1_1_1, hp_r1_1_2, hp_r1_1_3}; HST(sHS1 + 2048, hv_r1_1);
    MFMA(c3_0_0, a3_14_0, X0_14); SCHED();
    MFMA(c3_0_1, a3_14_0, X1_14); SCHED();
    const u32x4 a3_16_1 = LDSR(pW_2, 9216);
    float t16a; V_FMA(t16a, M0, v1_r2_0_0[0], v2_r2_0_0[0]);
    float t16b; V_FMA(t16b, M0, v1_r2_0_0[1], v2_r2_0_0[1]);
    MFMA(c3_1_0, a3_14_1, X0_14); SCHED();
    V_FMAC(t16a, c2_0_0[0], R0);
    V_FMAC(t16b, c2_0_0[1], R0);
    MFMA(c3_1_1, a3_14_1, X1_14); SCHED();
    const u32x4 a3_17_0 = LDSR(pW_2, 12288);
    float e16a; V_EXP(e16a, t16a);
    float e16b; V_EXP(e16b, t16b);
    MFMA(c3_0_0, a3_15_0, X0_15); SCHED();
    V_ADD1(e16a);
    V_ADD1(e16b);
    MFMA(c3_0_1, a3_15_0, X1_15); SCHED();
    const u32x4 a3_17_1 = LDSR(pW_2, 13312);
    V_RCP(e16a);
    V_RCP(e16b);
    MFMA(c3_1_0, a3_15_1, X0_15); SCHED();
    V_MUL(t16a, e16a);
    MFMA(c3_1_1, a3_15_1, X1_15); SCHED();
    const u32x4 a8_0_0 = LDSR(pW_0, 0);
    V_MUL(t16b, e16b);
    u32 hp_r2_0_0; V_CVT(hp_r2_0_0, t16a, t16b);
    DMA2(wsrc, 409600, 114688);
    MFMA(c3_0_0, a3_16_0, X0_16); SCHED();
    const u32x4 a8_0_1 = LDSR(pW_0, 1024);
    float t17a; V_FMA(t17a, M0, v1_r2_0_0[2], v2_r2_0_0[2]);
    float t17b; V_FMA(t17b, M0, v1_r2_0_0[3], v2_r2_0_0[3]);
    MFMA(c3_0_1, a3_16_0, X1_16); SCHED();
    const f32x4 v1_r2_0_1 = LDSRF(pV, 576), v2_r2_0_1 = LDSRF(pV, 1728);
    const u32x4 a8_1_0 = LDSR(pW_0, 2048);
    V_FMAC(t17a, c2_0_0[2], R0);
    V_FMAC(t17b, c2_0_0[3], R0);
    MFMA(c3_1_0, a3_16_1, X0_16); SCHED();
    const u32x4 a8_1_1 = LDSR(pW_0, 3072);
    float e17a; V_EXP(e17a, t17a);
    float e17b; V_EXP(e17b, t17b);
    MFMA(c3_1_1, a3_16_1, X1_16); SCHED();
    const u32x4 a8_2_0 = LDSR(pW_0, 4096);
    V_ADD1(e17a);
    V_ADD1(e17b);
    MFMA(c3_0_0, a3_17_0, X0_17); SCHED();
    const u32x4 a8_2_1 = LDSR(pW_0, 5120);
    V_RCP(e17a);
    MFMA(c3_0_1, a3_17_0, X1_17); SCHED();
    const u32x4 a8_3_0 = LDSR(pW_0, 6144);
    V_RCP(e17b);
    V_MUL(t17a, e17a);
    MFMA(c3_1_0, a3_17_1, X0_17); SCHED();
    const u32x4 a8_3_1 = LDSR(pW_0, 7168);
    V_MUL(t17b, e17b);
    u32 hp_r2_0_1; V_CVT(hp_r2_0_1, t17a, t17b);
    MFMA(c3_1_1, a3_17_1, X1_17); SCHED();
    WAITV(16); BAR();
    const u32x4 a8_4_0 = LDSR(pW_0, 8192);
    float t18a; V_FMA(t18a, M0, v1_r2_0_1[0], v2_r2_0_1[0]);
    float t18b; V_FMA(t18b, M0, v1_r2_0_1[1], v2_r2_0_1[1]);
    f32x4 c8_0; MFMA0(c8_0, a8_0_0, X0_0); SCHED();
    const u32x4 a8_4_1 = LDSR(pW_0, 9216);
    V_FMAC(t18a, c2_1_0[0], R0);
    V_FMAC(t18b, c2_1_0[1], R0);
    asm volatile("s_waitcnt vmcnt(3)" : "+v"(fl1)::"memory"); CHECK(fl1, 1u, sFL, 2);
    f32x4 c8_1; MFMA0(c8_1, a8_0_1, X0_0); SCHED();
    const u32x4 a8_5_0 = LDSR(pW_0, 10240);
    float e18a; V_EXP(e18a, t18a);
    float e18b; V_EXP(e18b, t18b);
    u32x4 h0_0; HLOAD(h0_0, sHJ0 + sHR0 + 0);
    MFMA(c8_0, a8_1_0, X0_1); SCHED();
    const u32x4 a8_5_1 = LDSR(pW_0, 11264);
    V_ADD1(e18a);
    u32x4 h0_1; HLOAD(h0_1, sHJ0 + sHR1 + 0);
    MFMA(c8_1, a8_1_1, X0_1); SCHED();
    const u32x4 a8_6_0 = LDSR(pW_0, 12288);
    V_ADD1(e18b);
    V_RCP(e18a);
    MFMA(c8_0, a8_2_0, X0_2); SCHED();
    const u32x4 a8_6_1 = LDSR(pW_0, 13312);
    V_RCP(e18b);
    V_MUL(t18a, e18a);
    MFMA(c8_1, a8_2_1, X0_2); SCHED();
    const u32x4 a8_7_0 = LDSR(pW_0, 14336);
    V_MUL(t18b, e18b);
    u32 hp_r2_0_2; V_CVT(hp_r2_0_2, t18a, t18b);
    MFMA(c8_0, a8_3_0, X0_3); SCHED();
    const u32x4 a8_7_1 = LDSR(pW_0, 15360);
    float t19a; V_FMA(t19a, M0, v1_r2_0_1[2], v2_r2_0_1[2]);
    float t19b; V_FMA(t19b, M0, v1_r2_0_1[3], v2_r2_0_1[3]);
    MFMA(c8_1, a8_3_1, X0_3); SCHED();
    const f32x4 v1_r2_1_0 = LDSRF(pV, 512), v2_r2_1_0 = LDSRF(pV, 1664);
    const u32x4 a8_8_0 = LDSR(pW_0, 16384);
    V_FMAC(t19a, c2_1_0[2], R0);
    MFMA(c8_0, a8_4_0, X0_4); SCHED();
    const u32x4 a8_8_1 = LDSR(pW_0, 17408);
    V_FMAC(t19b, c2_1_0[3], R0);
    float e19a; V_EXP(e19a, t19a);
    MFMA(c8_1, a8_4_1, X0_4); SCHED();
    const u32x4 a8_9_0 = LDSR(pW_0, 18432);
    float e19b; V_EXP(e19b, t19b);
    V_ADD1(e19a);
    u32x4 h1_0; HLOAD(h1_0, sHJ1 + sHR0 + 0);
    MFMA(c8_0, a8_5_0, X0_5); SCHED();
    const u32x4 a8_9_1 = LDSR(pW_0, 19456);
    V_ADD1(e19b);
    V_RCP(e19a);
    u32x4 h1_1; HLOAD(h1_1, sHJ1 + sHR1 + 0);
    MFMA(c8_1, a8_5_1, X0_5); SCHED();
    const u32x4 a8_10_0 = LDSR(pW_0, 20480);
    V_RCP(e19b);
    V_MUL(t19a, e19a);
    MFMA(c8_0, a8_6_0, X0_6); SCHED();
    const u32x4 a8_10_1 = LDSR(pW_0, 21504);
    V_MUL(t19b, e19b);
    u32 hp_r2_0_3; V_CVT(hp_r2_0_3, t19a, t19b);
    MFMA(c8_1, a8_6_1, X0_6); SCHED();
    const u32x4 a8_11_0 = LDSR(pW_0, 22528);
    float t20a; V_FMA(t20a, M1, v1_r2_1_0[0], v2_r2_1_0[0]);
    const u32x4 h8_0 = {hp_r2_0_0, hp_r2_0_1, hp_r2_0_2, hp_r2_0_3}; HST(sHS0 + 4096, h8_0);
    MFMA(c8_0, a8_7_0, X0_7); SCHED();
    const u32x4 a8_11_1 = LDSR(pW_0, 23552);
    float t20b; V_FMA(t20b, M1, v1_r2_1_0[1], v2_r2_1_0[1]);
    V_FMAC(t20a, c2_0_1[0], R1);
    MFMA(c8_1, a8_7_1, X0_7); SCHED();
    const u32x4 a8_12_0 = LDSR(pW_0, 24576);
    V_FMAC(t20b, c2_0_1[1], R1);
    float e20a; V_EXP(e20a, t20a);
    MFMA(c8_0, a8_8_0, X0_8); SCHED();
    const u32x4 a8_12_1 = LDSR(pW_0, 25600);
    float e20b; V_EXP(e20b, t20b);
    V_ADD1(e20a);
    MFMA(c8_1, a8_8_1, X0_8); SCHED();
    WAITV(7); BAR();
    const u32x4 a8_13_0 = LDSR(pW_0, 26624);
    V_ADD1(e20b);
    V_RCP(e20a);
    MFMA(c8_0, a8_9_0, X0_9); SCHED();
    if (rg == 0) { if (l16 == 0) __builtin_amdgcn_raw_buffer_store_b32(2u, rs, 0, (int)(sFL + j * 64), 16); }
    const u32x4 a8_13_1 = LDSR(pW_0, 27648);
    V_RCP(e20b);
    V_MUL(t20a, e20a);
    MFMA(c8_1, a8_9_1, X0_9); SCHED();
    const u32x4 a8_14_0 = LDSR(pW_0, 28672);
    V_MUL(t20b, e20b);
    DMA2(wsrc, 425984, 131072);
    MFMA(c8_0, a8_10_0, X0_10); SCHED();
    const u32x4 a8_14_1 = LDSR(pW_0, 29696);
    u32 hp_r2_1_0; V_CVT(hp_r2_1_0, t20a, t20b);
    float t21a; V_FMA(t21a, M1, v1_r2_1_0[2], v2_r2_1_0[2]);
    MFMA(c8_1, a8_10_1, X0_10); SCHED();
    const f32x4 v1_r2_1_1 = LDSRF(pV, 576), v2_r2_1_1 = LDSRF(pV, 1728);
    const u32x4 a8_15_0 = LDSR(pW_0, 30720);
    float t21b; V_FMA(t21b, M1, v1_r2_1_0[3], v2_r2_1_0[3]);
    V_FMAC(t21a, c2_0_1[2], R1);
    MFMA(c8_0, a8_11_0, X0_11); SCHED();
    const u32x4 a8_15_1 = LDSR(pW_0, 31744);
    V_FMAC(t21b, c2_0_1[3], R1);
    float e21a; V_EXP(e21a, t21a);
    MFMA(c8_1, a8_11_1, X0_11); SCHED();
    const u32x4 a8_16_0 = LDSR(pW_0, 32768);
    float e21b; V_EXP(e21b, t21b);
    V_ADD1(e21a);
    MFMA(c8_0, a8_12_0, X0_12); SCHED();
    const u32x4 a8_16_1 = LDSR(pW_0, 33792);
    V_ADD1(e21b);
    MFMA(c8_1, a8_12_1, X0_12); SCHED();
    const u32x4 a8_17_0 = LDSR(pW_0, 34816);
    V_RCP(e21a);
    V_RCP(e21b);
    MFMA(c8_0, a8_13_0, X0_13); SCHED();
    const u32x4 a8_17_1 = LDSR(pW_0, 35840);
    V_MUL(t21a, e21a);
    V_MUL(t21b, e21b);
    MFMA(c8_1, a8_13_1, X0_13); SCHED();
    const u32x4 w0_0 = LDSR(pW_0, 36864);
    u32 hp_r2_1_1; V_CVT(hp_r2_1_1, t21a, t21b);
    float t22a; V_FMA(t22a, M1, v1_r2_1_1[0], v2_r2_1_1[0]);
    u32x4 h2_0; HLOAD(h2_0, sHJ2 + sHR0 + 0);
    MFMA(c8_0, a8_14_0, X0_14); SCHED();
    float t22b; V_FMA(t22b, M1, v1_r2_1_1[1], v2_r2_1_1[1]);
    V_FMAC(t22a, c2_1_1[0], R1);
    u32x4 h2_1; HLOAD(h2_1, sHJ2 + sHR1 + 0);
    MFMA(c8_1, a8_14_1, X0_14); SCHED();
    const u32x4 w0_1 = LDSR(pW_0, 38912);
    V_FMAC(t22b, c2_1_1[1], R1);
    float e22a; V_EXP(e22a, t22a);
    MFMA(c8_0, a8_15_0, X0_15); SCHED();
    float e22b; V_EXP(e22b, t22b);
    MFMA(c8_1, a8_15_1, X0_15); SCHED();
    const u32x4 w0_2 = LDSR(pW_0, 40960);
    V_ADD1(e22a);
    V_ADD1(e22b);
    MFMA(c8_0, a8_16_0, X0_16); SCHED();
    V_RCP(e22a);
    V_RCP(e22b);
    MFMA(c8_1, a8_16_1, X0_16); SCHED();
    const u32x4 w0_3 = LDSR(pW_0, 43008);
    V_MUL(t22a, e22a);
    V_MUL(t22b, e22b);
    MFMA(c8_0, a8_17_0, X0_17); SCHED();
    u32 hp_r2_1_2; V_CVT(hp_r2_1_2, t22a, t22b);
    float t23a; V_FMA(t23a, M1, v1_r2_1_1[2], v2_r2_1_1[2]);
    MFMA(c8_1, a8_17_1, X0_17); SCHED();
    WAITV(18); BAR();
    const u32x4 w0_4 = LDSR(pW_0, 45056);
    float t23b; V_FMA(t23b, M1, v1_r2_1_1[3], v2_r2_1_1[3]);
    V_FMAC(t23a, c2_1_1[2], R1);
    asm volatile("s_waitcnt vmcnt(7)" : "+v"(h0_0), "+v"(h0_1)::"memory");
    f32x4 o0_0; MFMA0(o0_0, w0_0, h0_0); SCHED();
    V_FMAC(t23b, c2_1_1[3], R1);
    f32x4 o0_1; MFMA0(o0_1, w0_0, h0_1); SCHED();
    const u32x4 w0_5 = LDSR(pW_0, 47104);
    float e23a; V_EXP(e23a, t23a);
    float e23b; V_EXP(e23b, t23b);
    DMA2(wsrc, 442368, 0);
    f32x4 o1_0; MFMA0(o1_0, w0_1, h0_0); SCHED();
    V_ADD1(e23a);
    V_ADD1(e23b);
    f32x4 o1_1; MFMA0(o1_1, w0_1, h0_1); SCHED();
    const u32x4 w0_6 = LDSR(pW_0, 49152);
    V_RCP(e23a);
    V_RCP(e23b);
    f32x4 o2_0; MFMA0(o2_0, w0_2, h0_0); SCHED();
    const f32x4 v1_r3_0_0 = LDSRF(pV, 768), v2_r3_0_0 = LDSRF(pV, 1920);
    V_MUL(t23a, e23a);
    V_MUL(t23b, e23b);
    f32x4 o2_1; MFMA0(o2_1, w0_2, h0_1); SCHED();
    const u32x4 w0_7 = LDSR(pW_0, 51200);
    u32 hp_r2_1_3; V_CVT(hp_r2_1_3, t23a, t23b);
    u32 fl2; POLL(fl2, sFL, 2);
    f32x4 o3_0; MFMA0(o3_0, w0_3, h0_0); SCHED();
    const u32x4 h8_1 = {hp_r2_1_0, hp_r2_1_1, hp_r2_1_2, hp_r2_1_3}; HST(sHS1 + 4096, h8_1);
    f32x4 o3_1; MFMA0(o3_1, w0_3, h0_1); SCHED();
    const u32x4 w0_8 = LDSR(pW_0, 53248);
    f32x4 o4_0; MFMA0(o4_0, w0_4, h0_0); SCHED();
    float t24a; V_FMA(t24a, M0, v1_r3_0_0[0], v2_r3_0_0[0]);
    float t24b; V_FMA(t24b, M0, v1_r3_0_0[1], v2_r3_0_0[1]);
    f32x4 o4_1; MFMA0(o4_1, w0_4, h0_1); SCHED();
    const u32x4 w1_0 = LDSR(pW_0, 55296);
    V_FMAC(t24a, c3_0_0[0], R0);
    V_FMAC(t24b, c3_0_0[1], R0);
    u32x4 h3_0; HLOAD(h3_0, sHJ3 + sHR0 + 0);
    f32x4 o5_0; MFMA0(o5_0, w0_5, h0_0); SCHED();
    float e24a; V_EXP(e24a, t24a);
    float e24b; V_EXP(e24b, t24b);
    u32x4 h3_1; HLOAD(h3_1, sHJ3 + sHR1 + 0);
    f32x4 o5_1; MFMA0(o5_1, w0_5, h0_1); SCHED();
    const u32x4 w1_1 = LDSR(pW_0, 57344);
    V_ADD1(e24a);
    V_ADD1(e24b);
    f32x4 o6_0; MFMA0(o6_0, w0_6, h0_0); SCHED();
    V_RCP(e24a);
    V_RCP(e24b);
    f32x4 o6_1; MFMA0(o6_1, w0_6, h0_1); SCHED();
    const u32x4 w1_2 = LDSR(pW_0, 59392);
    V_MUL(t24a, e24a);
    f32x4 o7_0; MFMA0(o7_0, w0_7, h0_0); SCHED();
    V_MUL(t24b, e24b);
    u32 hp_r3_0_0; V_CVT(hp_r3_0_0, t24a, t24b);
    f32x4 o7_1; MFMA0(o7_1, w0_7, h0_1); SCHED();
    const u32x4 w1_3 = LDSR(pW_0, 61440);
    float t25a; V_FMA(t25a, M0, v1_r3_0_0[2], v2_r3_0_0[2]);
    float t25b; V_FMA(t25b, M0, v1_r3_0_0[3], v2_r3_0_0[3]);
    f32x4 o8_0; MFMA0(o8_0, w0_8, h0_0); SCHED();
    const f32x4 v1_r3_0_1 = LDSRF(pV, 832), v2_r3_0_1 = LDSRF(pV, 1984);
    V_FMAC(t25a, c3_0_0[2], R0);
    V_FMAC(t25b, c3_0_0[3], R0);
    f32x4 o8_1; MFMA0(o8_1, w0_8, h0_1); SCHED();
    const u32x4 w1_4 = LDSR(pW_0, 63488);
    float e25a; V_EXP(e25a, t25a);
    float e25b; V_EXP(e25b, t25b);
    asm volatile("s_waitcnt vmcnt(11)" : "+v"(h1_0), "+v"(h1_1)::"memory");
    MFMA(o0_0, w1_0, h1_0); SCHED();
    V_ADD1(e25a);
    V_ADD1(e25b);
    DMA2(wsrc, 458752, 16384);
    MFMA(o0_1, w1_0, h1_1); SCHED();
    const u32x4 w1_5 = LDSR(pW_1, 0);
    V_RCP(e25a);
    MFMA(o1_0, w1_1, h1_0); SCHED();
    V_RCP(e25b);
    V_MUL(t25a, e25a);
    MFMA(o1_1, w1_1, h1_1); SCHED();
    const u32x4 w1_6 = LDSR(pW_1, 2048);
    V_MUL(t25b, e25b);
    u32 hp_r3_0_1; V_CVT(hp_r3_0_1, t25a, t25b);
    MFMA(o2_0, w1_2, h1_0); SCHED();
    float t26a; V_FMA(t26a, M0, v1_r3_0_1[0], v2_r3_0_1[0]);
    float t26b; V_FMA(t26b, M0, v1_r3_0_1[1], v2_r3_0_1[1]);
    MFMA(o2_1, w1_2, h1_1); SCHED();
    const u32x4 w1_7 = LDSR(pW_1, 4096);
    V_FMAC(t26a, c3_1_0[0], R0);
    V_FMAC(t26b, c3_1_0[1], R0);
    MFMA(o3_0, w1_3, h1_0); SCHED();
    float e26a; V_EXP(e26a, t26a);
    float e26b; V_EXP(e26b, t26b);
    MFMA(o3_1, w1_3, h1_1); SCHED();
    const u32x4 w1_8 = LDSR(pW_1, 6144);
    V_ADD1(e26a);
    MFMA(o4_0, w1_4, h1_0); SCHED();
    V_ADD1(e26b);
    V_RCP(e26a);
    asm volatile("s_waitcnt vmcnt(5)" : "+v"(fl2)::"memory"); CHECK(fl2, 2u, sFL, 2);
    MFMA(o4_1, w1_4, h1_1); SCHED();
    const u32x4 w2_0 = LDSR(pW_1, 8192);
    V_RCP(e26b);
    V_MUL(t26a, e26a);
    u32x4 h4_0; HLOAD(h4_0, sHJ0 + sHR0 + 2048);
    MFMA(o5_0, w1_5, h1_0); SCHED();
    V_MUL(t26b, e26b);
    u32 hp_r3_0_2; V_CVT(hp_r3_0_2, t26a, t26b);
    u32x4 h4_1; HLOAD(h4_1, sHJ0 + sHR1 + 2048);
    MFMA(o5_1, w1_5, h1_1); SCHED();
    const u32x4 w2_1 = LDSR(pW_1, 10240);
    float t27a; V_FMA(t27a, M0, v1_r3_0_1[2], v2_r3_0_1[2]);
    float t27b; V_FMA(t27b, M0, v1_r3_0_1[3], v2_r3_0_1[3]);
    MFMA(o6_0, w1_6, h1_0); SCHED();
    const f32x4 v1_r3_1_0 = LDSRF(pV, 768), v2_r3_1_0 = LDSRF(pV, 1920);
    V_FMAC(t27a, c3_1_0[2], R0);
    MFMA(o6_1, w1_6, h1_1); SCHED();
    const u32x4 w2_2 = LDSR(pW_1, 12288);
    V_FMAC(t27b, c3_1_0[3], R0);
    float e27a; V_EXP(e27a, t27a);
    MFMA(o7_0, w1_7, h1_0); SCHED();
    float e27b; V_EXP(e27b, t27b);
    V_ADD1(e27a);
    MFMA(o7_1, w1_7, h1_1); SCHED();
    const u32x4 w2_3 = LDSR(pW_1, 14336);
    V_ADD1(e27b);
    V_RCP(e27a);
    MFMA(o8_0, w1_8, h1_0); SCHED();
    V_RCP(e27b);
    V_MUL(t27a, e27a);
    MFMA(o8_1, w1_8, h1_1); SCHED();
    WAITV(26); BAR();
    const u32x4 w2_4 = LDSR(pW_1, 16384);
    V_MUL(t27b, e27b);
    u32 hp_r3_0_3; V_CVT(hp_r3_0_3, t27a, t27b);
    asm volatile("s_waitcnt vmcnt(10)" : "+v"(h2_0), "+v"(h2_1)::"memory");
    MFMA(o0_0, w2_0, h2_0); SCHED();
    float t28a; V_FMA(t28a, M1, v1_r3_1_0[0], v2_r3_1_0[0]);
    const u32x4 h12_0 = {hp_r3_0_0, hp_r3_0_1, hp_r3_0_2, hp_r3_0_3}; HST(sHS0 + 6144, h12_0);
    MFMA(o0_1, w2_0, h2_1); SCHED();
    const u32x4 w2_5 = LDSR(pW_1, 18432);
    float t28b; V_FMA(t28b, M1, v1_r3_1_0[1], v2_r3_1_0[1]);
    V_FMAC(t28a, c3_0_1[0], R1);
    DMA2(wsrc, 475136, 32768);
    MFMA(o1_0, w2_1, h2_0); SCHED();
    V_FMAC(t28b, c3_0_1[1], R1);
    float e28a; V_EXP(e28a, t28a);
    MFMA(o1_1, w2_1, h2_1); SCHED();
    const u32x4 w2_6 = LDSR(pW_1, 20480);
    float e28b; V_EXP(e28b, t28b);
    V_ADD1(e28a);
    MFMA(o2_0, w2_2, h2_0); SCHED();
    V_ADD1(e28b);
    V_RCP(e28a);
    MFMA(o2_1, w2_2, h2_1); SCHED();
    const u32x4 w2_7 = LDSR(pW_1, 22528);
    V_RCP(e28b);
    V_MUL(t28a, e28a);
    MFMA(o3_0, w2_3, h2_0); SCHED();
    V_MUL(t28b, e28b);
    MFMA(o3_1, w2_3, h2_1); SCHED();
    WAITV(9); BAR();
    const u32x4 w2_8 = LDSR(pW_1, 24576);
    u32 hp_r3_1_0; V_CVT(hp_r3_1_0, t28a, t28b);
    float t29a; V_FMA(t29a, M1, v1_r3_1_0[2], v2_r3_1_0[2]);
    MFMA(o4_0, w2_4, h2_0); SCHED();
    if (rg == 0) { if (l16 == 0) __builtin_amdgcn_raw_buffer_store_b32(3u, rs, 0, (int)(sFL + j * 64), 16); }
    const f32x4 v1_r3_1_1 = LDSRF(pV, 832), v2_r3_1_1 = LDSRF(pV, 1984);
    float t29b; V_FMA(t29b, M1, v1_r3_1_0[3], v2_r3_1_0[3]);
    V_FMAC(t29a, c3_0_1[2], R1);
    MFMA(o4_1, w2_4, h2_1); SCHED();
    const u32x4 w3_0 = LDSR(pW_1, 26624);
    V_FMAC(t29b, c3_0_1[3], R1);
    float e29a; V_EXP(e29a, t29a);
    u32x4 h5_0; HLOAD(h5_0, sHJ1 + sHR0 + 2048);
    MFMA(o5_0, w2_5, h2_0); SCHED();
    float e29b; V_EXP(e29b, t29b);
    V_ADD1(e29a);
    u32x4 h5_1; HLOAD(h5_1, sHJ1 + sHR1 + 2048);
    MFMA(o5_1, w2_5, h2_1); SCHED();
    const u32x4 w3_1 = LDSR(pW_1, 28672);
    V_ADD1(e29b);
    MFMA(o6_0, w2_6, h2_0); SCHED();
    V_RCP(e29a);
    V_RCP(e29b);
    MFMA(o6_1, w2_6, h2_1); SCHED();
    const u32x4 w3_2 = LDSR(pW_1, 30720);
    V_MUL(t29a, e29a);
    V_MUL(t29b, e29b);
    MFMA(o7_0, w2_7, h2_0); SCHED();
    u32 hp_r3_1_1; V_CVT(hp_r3_1_1, t29a, t29b);
    float t30a; V_FMA(t30a, M1, v1_r3_1_1[0], v2_r3_1_1[0]);
    MFMA(o7_1, w2_7, h2_1); SCHED();
    const u32x4 w3_3 = LDSR(pW_1, 32768);
    float t30b; V_FMA(t30b, M1, v1_r3_1_1[1], v2_r3_1_1[1]);
    V_FMAC(t30a, c3_1_1[0], R1);
    MFMA(o8_0, w2_8, h2_0); SCHED();
    V_FMAC(t30b, c3_1_1[1], R1);
    float e30a; V_EXP(e30a, t30a);
    MFMA(o8_1, w2_8, h2_1); SCHED();
    const u32x4 w3_4 = LDSR(pW_1, 34816);
    float e30b; V_EXP(e30b, t30b);
    DMA2(wsrc, 491520, 49152);
    asm volatile("s_waitcnt vmcnt(11)" : "+v"(h3_0), "+v"(h3_1)::"memory");
    MFMA(o0_0, w3_0, h3_0); SCHED();
    V_ADD1(e30a);
    V_ADD1(e30b);
    MFMA(o0_1, w3_0, h3_1); SCHED();
    const u32x4 w3_5 = LDSR(pW_1, 36864);
    V_RCP(e30a);
    V_RCP(e30b);
    MFMA(o1_0, w3_1, h3_0); SCHED();
    V_MUL(t30a, e30a);
    V_MUL(t30b, e30b);
    MFMA(o1_1, w3_1, h3_1); SCHED();
    const u32x4 w3_6 = LDSR(pW_1, 38912);
    u32 hp_r3_1_2; V_CVT(hp_r3_1_2, t30a, t30b);
    float t31a; V_FMA(t31a, M1, v1_r3_1_1[2], v2_r3_1_1[2]);
    MFMA(o2_0, w3_2, h3_0); SCHED();
    float t31b; V_FMA(t31b, M1, v1_r3_1_1[3], v2_r3_1_1[3]);
    V_FMAC(t31a, c3_1_1[2], R1);
    MFMA(o2_1, w3_2, h3_1); SCHED();
    const u32x4 w3_7 = LDSR(pW_1, 40960);
    V_FMAC(t31b, c3_1_1[3], R1);
    MFMA(o3_0, w3_3, h3_0); SCHED();
    float e31a; V_EXP(e31a, t31a);
    float e31b; V_EXP(e31b, t31b);
    MFMA(o3_1, w3_3, h3_1); SCHED();
    const u32x4 w3_8 = LDSR(pW_1, 43008);
    V_ADD1(e31a);
    V_ADD1(e31b);
    MFMA(o4_0, w3_4, h3_0); SCHED();
    V_RCP(e31a);
    V_RCP(e31b);
    MFMA(o4_1, w3_4, h3_1); SCHED();
    const f32x4 v1_b8_0_0 = LDSRF(pV, 1024), v2_b8_0_0 = LDSRF(pV, 2176);
    const u32x4 w4_0 = LDSR(pW_1, 45056);
    V_MUL(t31a, e31a);
    V_MUL(t31b, e31b);
    u32x4 h6_0; HLOAD(h6_0, sHJ2 + sHR0 + 2048);
    MFMA(o5_0, w3_5, h3_0); SCHED();
    u32 hp_r3_1_3; V_CVT(hp_r3_1_3, t31a, t31b);
    u32x4 h6_1; HLOAD(h6_1, sHJ2 + sHR1 + 2048);
    MFMA(o5_1, w3_5, h3_1); SCHED();
    const u32x4 w4_1 = LDSR(pW_1, 47104);
    const u32x4 h12_1 = {hp_r3_1_0, hp_r3_1_1, hp_r3_1_2, hp_r3_1_3}; HST(sHS1 + 6144, h12_1);
    MFMA(o6_0, w3_6, h3_0); SCHED();
    MFMA(o6_1, w3_6, h3_1); SCHED();
    const u32x4 w4_2 = LDSR(pW_1, 49152);
    float t32a; V_FMA(t32a, M0, v1_b8_0_0[0], v2_b8_0_0[0]);
    float t32b; V_FMA(t32b, M0, v1_b8_0_0[1], v2_b8_0_0[1]);
    V_FMAC(t32a, c8_0[0], R0);
    MFMA(o7_0, w3_7, h3_0); SCHED();
    V_FMAC(t32b, c8_0[1], R0);
    float e32a; V_EXP(e32a, t32a);
    MFMA(o7_1, w3_7, h3_1); SCHED();
    const u32x4 w4_3 = LDSR(pW_1, 51200);
    float e32b; V_EXP(e32b, t32b);
    V_ADD1(e32a);
    MFMA(o8_0, w3_8, h3_0); SCHED();
    V_ADD1(e32b);
    V_RCP(e32a);
    MFMA(o8_1, w3_8, h3_1); SCHED();
    WAITV(18); BAR();
    const u32x4 w4_4 = LDSR(pW_1, 53248);
    V_RCP(e32b);
    V_MUL(t32a, e32a);
    asm volatile("s_waitcnt vmcnt(10)" : "+v"(h4_0), "+v"(h4_1)::"memory");
    MFMA(o0_0, w4_0, h4_0); SCHED();
    V_MUL(t32b, e32b);
    u32 hp_b8_0_0; V_CVT(hp_b8_0_0, t32a, t32b);
    MFMA(o0_1, w4_0, h4_1); SCHED();
    const f32x4 v1_b8_0_1 = LDSRF(pV, 1088), v2_b8_0_1 = LDSRF(pV, 2240);
    const u32x4 w4_5 = LDSR(pW_1, 55296);
    float t33a; V_FMA(t33a, M0, v1_b8_0_0[2], v2_b8_0_0[2]);
    float t33b; V_FMA(t33b, M0, v1_b8_0_0[3], v2_b8_0_0[3]);
    DMA2(wsrc, 507904, 65536);
    MFMA(o1_0, w4_1, h4_0); SCHED();
    V_FMAC(t33a, c8_0[2], R0);
    V_FMAC(t33b, c8_0[3], R0);
    MFMA(o1_1, w4_1, h4_1); SCHED();
    const u32x4 w4_6 = LDSR(pW_1, 57344);
    float e33a; V_EXP(e33a, t33a);
    float e33b; V_EXP(e33b, t33b);
    MFMA(o2_0, w4_2, h4_0); SCHED();
    V_ADD1(e33a);
    V_ADD1(e33b);
    MFMA(o2_1, w4_2, h4_1); SCHED();
    const u32x4 w4_7 = LDSR(pW_1, 59392);
    V_RCP(e33a);
    V_RCP(e33b);
    MFMA(o3_0, w4_3, h4_0); SCHED();
    V_MUL(t33a, e33a);
    V_MUL(t33b, e33b);
    MFMA(o3_1, w4_3, h4_1); SCHED();
    const u32x4 w4_8 = LDSR(pW_1, 61440);
    u32 hp_b8_0_1; V_CVT(hp_b8_0_1, t33a, t33b);
    float t34a; V_FMA(t34a, M0, v1_b8_0_1[0], v2_b8_0_1[0]);
    float t34b; V_FMA(t34b, M0, v1_b8_0_1[1], v2_b8_0_1[1]);
    MFMA(o4_0, w4_4, h4_0); SCHED();
    V_FMAC(t34a, c8_1[0], R0);
    V_FMAC(t34b, c8_1[1], R0);
    MFMA(o4_1, w4_4, h4_1); SCHED();
    const u32x4 w5_0 = LDSR(pW_1, 63488);
    float e34a; V_EXP(e34a, t34a);
    float e34b; V_EXP(e34b, t34b);
    u32x4 h7_0; HLOAD(h7_0, sHJ3 + sHR0 + 2048);
    MFMA(o5_0, w4_5, h4_0); SCHED();
    V_ADD1(e34a);
    V_ADD1(e34b);
    u32x4 h7_1; HLOAD(h7_1, sHJ3 + sHR1 + 2048);
    MFMA(o5_1, w4_5, h4_1); SCHED();
    const u32x4 w5_1 = LDSR(pW_2, 0);
    V_RCP(e34a);
    V_RCP(e34b);
    MFMA(o6_0, w4_6, h4_0); SCHED();
    V_MUL(t34a, e34a);
    V_MUL(t34b, e34b);
    MFMA(o6_1, w4_6, h4_1); SCHED();
    const u32x4 w5_2 = LDSR(pW_2, 2048);
    u32 hp_b8_0_2; V_CVT(hp_b8_0_2, t34a, t34b);
    float t35a; V_FMA(t35a, M0, v1_b8_0_1[2], v2_b8_0_1[2]);
    MFMA(o7_0, w4_7, h4_0); SCHED();
    float t35b; V_FMA(t35b, M0, v1_b8_0_1[3], v2_b8_0_1[3]);
    V_FMAC(t35a, c8_1[2], R0);
    MFMA(o7_1, w4_7, h4_1); SCHED();
    const u32x4 w5_3 = LDSR(pW_2, 4096);
    V_FMAC(t35b, c8_1[3], R0);
    float e35a; V_EXP(e35a, t35a);
    MFMA(o8_0, w4_8, h4_0); SCHED();
    float e35b; V_EXP(e35b, t35b);
    V_ADD1(e35a);
    MFMA(o8_1, w4_8, h4_1); SCHED();
    const u32x4 w5_4 = LDSR(pW_2, 6144);
    V_ADD1(e35b);
    V_RCP(e35a);
    asm volatile("s_waitcnt vmcnt(9)" : "+v"(h5_0), "+v"(h5_1)::"memory");
    MFMA(o0_0, w5_0, h5_0); SCHED();
    V_RCP(e35b);
    V_MUL(t35a, e35a);
    DMA2(wsrc, 524288, 81920);
    MFMA(o0_1, w5_0, h5_1); SCHED();
    const u32x4 w5_5 = LDSR(pW_2, 8192);
    V_MUL(t35b, e35b);
    u32 hp_b8_0_3; V_CVT(hp_b8_0_3, t35a, t35b);
    u32 fl3; POLL(fl3, sFL, 2);
    MFMA(o1_0, w5_1, h5_0); SCHED();
    const u32x4 hv_b8_0 = {hp_b8_0_0, hp_b8_0_1, hp_b8_0_2, hp_b8_0_3}; HST(sHS0 + 8192, hv_b8_0);
    MFMA(o1_1, w5_1, h5_1); SCHED();
    const u32x4 w5_6 = LDSR(pW_2, 10240);
    MFMA(o2_0, w5_2, h5_0); SCHED();
    MFMA(o2_1, w5_2, h5_1); SCHED();
    const u32x4 w5_7 = LDSR(pW_2, 12288);
    MFMA(o3_0, w5_3, h5_0); SCHED();
    MFMA(o3_1, w5_3, h5_1); SCHED();
    const u32x4 w5_8 = LDSR(pW_2, 14336);
    MFMA(o4_0, w5_4, h5_0); SCHED();
    MFMA(o4_1, w5_4, h5_1); SCHED();
    const u32x4 w6_0 = LDSR(pW_0, 0);
    MFMA(o5_0, w5_5, h5_0); SCHED();
    MFMA(o5_1, w5_5, h5_1); SCHED();
    const u32x4 w6_1 = LDSR(pW_0, 2048);
    MFMA(o6_0, w5_6, h5_0); SCHED();
    MFMA(o6_1, w5_6, h5_1); SCHED();
    const u32x4 w6_2 = LDSR(pW_0, 4096);
    MFMA(o7_0, w5_7, h5_0); SCHED();
    MFMA(o7_1, w5_7, h5_1); SCHED();
    const u32x4 w6_3 = LDSR(pW_0, 6144);
    MFMA(o8_0, w5_8, h5_0); SCHED();
    MFMA(o8_1, w5_8, h5_1); SCHED();
    WAITV(20); BAR();
    const u32x4 w6_4 = LDSR(pW_0, 8192);
    asm volatile("s_waitcnt vmcnt(9)" : "+v"(h6_0), "+v"(h6_1)::"memory");
    MFMA(o0_0, w6_0, h6_0); SCHED();
    MFMA(o0_1, w6_0, h6_1); SCHED();
    const u32x4 w6_5 = LDSR(pW_0, 10240);
    DMA2(wsrc, 540672, 98304);
    MFMA(o1_0, w6_1, h6_0); SCHED();
    MFMA(o1_1, w6_1, h6_1); SCHED();
    const u32x4 w6_6 = LDSR(pW_0, 12288);
    MFMA(o2_0, w6_2, h6_0); SCHED();
    XRES0();
    asm volatile("s_waitcnt vmcnt(5)" : "+v"(fl3)::"memory"); CHECK(fl3, 3u, sFL, 2);
    MFMA(o2_1, w6_2, h6_1); SCHED();
    const u32x4 w6_7 = LDSR(pW_0, 14336);
    u32x4 h9_0; HLOAD(h9_0, sHJ1 + sHR0 + 4096);
    MFMA(o3_0, w6_3, h6_0); SCHED();
    u32x4 h9_1; HLOAD(h9_1, sHJ1 + sHR1 + 4096);
    MFMA(o3_1, w6_3, h6_1); SCHED();
    const u32x4 w6_8 = LDSR(pW_0, 16384);
    MFMA(o4_0, w6_4, h6_0); SCHED();
    MFMA(o4_1, w6_4, h6_1); SCHED();
    const u32x4 w7_0 = LDSR(pW_0, 18432);
    MFMA(o5_0, w6_5, h6_0); SCHED();
    MFMA(o5_1, w6_5, h6_1); SCHED();
    const u32x4 w7_1 = LDSR(pW_0, 20480);
    MFMA(o6_0, w6_6, h6_0); SCHED();
    MFMA(o6_1, w6_6, h6_1); SCHED();
    const u32x4 w7_2 = LDSR(pW_0, 22528);
    MFMA(o7_0, w6_7, h6_0); SCHED();
    MFMA(o7_1, w6_7, h6_1); SCHED();
    const u32x4 w7_3 = LDSR(pW_0, 24576);
    MFMA(o8_0, w6_8, h6_0); SCHED();
    MFMA(o8_1, w6_8, h6_1); SCHED();
    WAITV(6); BAR();
    const u32x4 w7_4 = LDSR(pW_0, 26624);
    asm volatile("s_waitcnt vmcnt(10)" : "+v"(h7_0), "+v"(h7_1)::"memory");
    MFMA(o0_0, w7_0, h7_0); SCHED();
    if (rg == 0) { if (l16 == 0) __builtin_amdgcn_raw_buffer_store_b32(4u, rs, 0, (int)(sFL + j * 64), 16); }
    MFMA(o0_1, w7_0, h7_1); SCHED();
    const u32x4 w7_5 = LDSR(pW_0, 28672);
    u32x4 h10_0; HLOAD(h10_0, sHJ2 + sHR0 + 4096);
    MFMA(o1_0, w7_1, h7_0); SCHED();
    u32x4 h10_1; HLOAD(h10_1, sHJ2 + sHR1 + 4096);
    MFMA(o1_1, w7_1, h7_1); SCHED();
    const u32x4 w7_6 = LDSR(pW_0, 30720);
    MFMA(o2_0, w7_2, h7_0); SCHED();
    MFMA(o2_1, w7_2, h7_1); SCHED();
    const u32x4 w7_7 = LDSR(pW_0, 32768);
    MFMA(o3_0, w7_3, h7_0); SCHED();
    MFMA(o3_1, w7_3, h7_1); SCHED();
    const u32x4 w7_8 = LDSR(pW_0, 34816);
    MFMA(o4_0, w7_4, h7_0); SCHED();
    MFMA(o4_1, w7_4, h7_1); SCHED();
    const u32x4 w8_0 = LDSR(pW_0, 36864);
    DMA2(wsrc, 557056, 114688);
    MFMA(o5_0, w7_5, h7_0); SCHED();
    MFMA(o5_1, w7_5, h7_1); SCHED();
    const u32x4 w8_1 = LDSR(pW_0, 38912);
    MFMA(o6_0, w7_6, h7_0); SCHED();
    MFMA(o6_1, w7_6, h7_1); SCHED();
    const u32x4 w8_2 = LDSR(pW_0, 40960);
    MFMA(o7_0, w7_7, h7_0); SCHED();
    MFMA(o7_1, w7_7, h7_1); SCHED();
    const u32x4 w8_3 = LDSR(pW_0, 43008);
    u32x4 h11_0; HLOAD(h11_0, sHJ3 + sHR0 + 4096);
    MFMA(o8_0, w7_8, h7_0); SCHED();
    u32x4 h11_1; HLOAD(h11_1, sHJ3 + sHR1 + 4096);
    MFMA(o8_1, w7_8, h7_1); SCHED();
    WAITV(18); BAR();
    const u32x4 w8_4 = LDSR(pW_0, 45056);
    MFMA(o0_0, w8_0, h8_0); SCHED();
    MFMA(o0_1, w8_0, h8_1); SCHED();
    const u32x4 w8_5 = LDSR(pW_0, 47104);
    MFMA(o1_0, w8_1, h8_0); SCHED();
    MFMA(o1_1, w8_1, h8_1); SCHED();
    const u32x4 w8_6 = LDSR(pW_0, 49152);
    u32 fl4; POLL(fl4, sFL, 2);
    MFMA(o2_0, w8_2, h8_0); SCHED();
    MFMA(o2_1, w8_2, h8_1); SCHED();
    const u32x4 w9_0 = LDSR(pW_0, 51200);
    MFMA(o3_0, w8_3, h8_0); SCHED();
    MFMA(o3_1, w8_3, h8_1); SCHED();
    const u32x4 w9_1 = LDSR(pW_0, 53248);
    MFMA(o4_0, w8_4, h8_0); SCHED();
    MFMA(o4_1, w8_4, h8_1); SCHED();
    const u32x4 w9_2 = LDSR(pW_0, 55296);
    DMA2(wsrc, 573440, 131072);
    MFMA(o5_0, w8_5, h8_0); SCHED();
    MFMA(o5_1, w8_5, h8_1); SCHED();
    const u32x4 w9_3 = LDSR(pW_0, 57344);
    MFMA(o6_0, w8_6, h8_0); SCHED();
    MFMA(o6_1, w8_6, h8_1); SCHED();
    const u32x4 w9_4 = LDSR(pW_0, 59392);
    asm volatile("s_waitcnt vmcnt(9)" : "+v"(h9_0), "+v"(h9_1)::"memory");
    MFMA(o0_0, w9_0, h9_0); SCHED();
    MFMA(o0_1, w9_0, h9_1); SCHED();
    const u32x4 w9_5 = LDSR(pW_0, 61440);
    MFMA(o1_0, w9_1, h9_0); SCHED();
    MFMA(o1_1, w9_1, h9_1); SCHED();
    const u32x4 w9_6 = LDSR(pW_0, 63488);
    MFMA(o2_0, w9_2, h9_0); SCHED();
    MFMA(o2_1, w9_2, h9_1); SCHED();
    const u32x4 w10_0 = LDSR(pW_1, 0);
    MFMA(o3_0, w9_3, h9_0); SCHED();
    MFMA(o3_1, w9_3, h9_1); SCHED();
    const u32x4 w10_1 = LDSR(pW_1, 2048);
    MFMA(o4_0, w9_4, h9_0); SCHED();
    MFMA(o4_1, w9_4, h9_1); SCHED();
    const u32x4 w10_2 = LDSR(pW_1, 4096);
    MFMA(o5_0, w9_5, h9_0); SCHED();
    asm volatile("s_waitcnt vmcnt(2)" : "+v"(fl4)::"memory"); CHECK(fl4, 4u, sFL, 2);
    MFMA(o5_1, w9_5, h9_1); SCHED();
    const u32x4 w10_3 = LDSR(pW_1, 6144);
    u32x4 h13_0; HLOAD(h13_0, sHJ1 + sHR0 + 6144);
    MFMA(o6_0, w9_6, h9_0); SCHED();
    DMA2(wsrc, 589824, 0);
    u32x4 h13_1; HLOAD(h13_1, sHJ1 + sHR1 + 6144);
    MFMA(o6_1, w9_6, h9_1); SCHED();
    const u32x4 w10_4 = LDSR(pW_1, 8192);
    asm volatile("s_waitcnt vmcnt(11)" : "+v"(h10_0), "+v"(h10_1)::"memory");
    MFMA(o0_0, w10_0, h10_0); SCHED();
    MFMA(o0_1, w10_0, h10_1); SCHED();
    const u32x4 w10_5 = LDSR(pW_1, 10240);
    MFMA(o1_0, w10_1, h10_0); SCHED();
    MFMA(o1_1, w10_1, h10_1); SCHED();
    const u32x4 w10_6 = LDSR(pW_1, 12288);
    MFMA(o2_0, w10_2, h10_0); SCHED();
    MFMA(o2_1, w10_2, h10_1); SCHED();
    const u32x4 w11_0 = LDSR(pW_1, 14336);
    MFMA(o3_0, w10_3, h10_0); SCHED();
    MFMA(o3_1, w10_3, h10_1); SCHED();
    WAITV(9); BAR();
    const u32x4 w11_1 = LDSR(pW_1, 16384);
    MFMA(o4_0, w10_4, h10_0); SCHED();
    MFMA(o4_1, w10_4, h10_1); SCHED();
    const u32x4 w11_2 = LDSR(pW_1, 18432);
    MFMA(o5_0, w10_5, h10_0); SCHED();
    MFMA(o5_1, w10_5, h10_1); SCHED();
    const u32x4 w11_3 = LDSR(pW_1, 20480);
    u32x4 h14_0; HLOAD(h14_0, sHJ2 + sHR0 + 6144);
    MFMA(o6_0, w10_6, h10_0); SCHED();
    u32x4 h14_1; HLOAD(h14_1, sHJ2 + sHR1 + 6144);
    MFMA(o6_1, w10_6, h10_1); SCHED();
    const u32x4 w11_4 = LDSR(pW_1, 22528);
    asm volatile("s_waitcnt vmcnt(9)" : "+v"(h11_0), "+v"(h11_1)::"memory");
    MFMA(o0_0, w11_0, h11_0); SCHED();
    MFMA(o0_1, w11_0, h11_1); SCHED();
    const u32x4 w11_5 = LDSR(pW_1, 24576);
    MFMA(o1_0, w11_1, h11_0); SCHED();
    MFMA(o1_1, w11_1, h11_1); SCHED();
    const u32x4 w11_6 = LDSR(pW_1, 26624);
    DMA2(wsrc, 606208, 16384);
    MFMA(o2_0, w11_2, h11_0); SCHED();
    MFMA(o2_1, w11_2, h11_1); SCHED();
    const u32x4 w12_0 = LDSR(pW_1, 28672);
    MFMA(o3_0, w11_3, h11_0); SCHED();
    MFMA(o3_1, w11_3, h11_1); SCHED();
    const u32x4 w12_1 = LDSR(pW_1, 30720);
    MFMA(o4_0, w11_4, h11_0); SCHED();
    MFMA(o4_1, w11_4, h11_1); SCHED();
    const u32x4 w12_2 = LDSR(pW_1, 32768);
    MFMA(o5_0, w11_5, h11_0); SCHED();
    MFMA(o5_1, w11_5, h11_1); SCHED();
    const u32x4 w12_3 = LDSR(pW_1, 34816);
    u32x4 h15_0; HLOAD(h15_0, sHJ3 + sHR0 + 6144);
    MFMA(o6_0, w11_6, h11_0); SCHED();
    u32x4 h15_1; HLOAD(h15_1, sHJ3 + sHR1 + 6144);
    MFMA(o6_1, w11_6, h11_1); SCHED();
    const u32x4 w12_4 = LDSR(pW_1, 36864);
    MFMA(o0_0, w12_0, h12_0); SCHED();
    MFMA(o0_1, w12_0, h12_1); SCHED();
    const u32x4 w12_5 = LDSR(pW_1, 38912);
    MFMA(o1_0, w12_1, h12_0); SCHED();
    u32x4 h16_0; HLOAD(h16_0, sHR0 + 8192);
    MFMA(o1_1, w12_1, h12_1); SCHED();
    const u32x4 w12_6 = LDSR(pW_1, 40960);
    u32x4 h16_1; HLOAD(h16_1, sHR1 + 8192);
    MFMA(o2_0, w12_2, h12_0); SCHED();
    MFMA(o2_1, w12_2, h12_1); SCHED();
    const u32x4 w13_0 = LDSR(pW_1, 43008);
    MFMA(o3_0, w12_3, h12_0); SCHED();
    DMA2(wsrc, 622592, 32768);
    MFMA(o3_1, w12_3, h12_1); SCHED();
    const u32x4 w13_1 = LDSR(pW_1, 45056);
    u32x4 h17_0; HLOAD(h17_0, sHR0 + 26624);
    MFMA(o4_0, w12_4, h12_0); SCHED();
    u32x4 h17_1; HLOAD(h17_1, sHR1 + 26624);
    MFMA(o4_1, w12_4, h12_1); SCHED();
    const u32x4 w13_2 = LDSR(pW_1, 47104);
    MFMA(o5_0, w12_5, h12_0); SCHED();
    MFMA(o5_1, w12_5, h12_1); SCHED();
    const u32x4 w13_3 = LDSR(pW_1, 49152);
    u32 fl5; POLL(fl5, sFQ, 4);
    MFMA(o6_0, w12_6, h12_0); SCHED();
    MFMA(o6_1, w12_6, h12_1); SCHED();
    const u32x4 w13_4 = LDSR(pW_1, 51200);
    asm volatile("s_waitcnt vmcnt(13)" : "+v"(h13_0), "+v"(h13_1)::"memory");
    MFMA(o0_0, w13_0, h13_0); SCHED();
    MFMA(o0_1, w13_0, h13_1); SCHED();
    WAITV(17); BAR();
    const u32x4 w13_5 = LDSR(pW_1, 53248);
    MFMA(o1_0, w13_1, h13_0); SCHED();
    MFMA(o1_1, w13_1, h13_1); SCHED();
    const u32x4 w13_6 = LDSR(pW_1, 55296);
    MFMA(o2_0, w13_2, h13_0); SCHED();
    MFMA(o2_1, w13_2, h13_1); SCHED();
    const u32x4 w14_0 = LDSR(pW_1, 57344);
    MFMA(o3_0, w13_3, h13_0); SCHED();
    MFMA(o3_1, w13_3, h13_1); SCHED();
    const u32x4 w14_1 = LDSR(pW_1, 59392);
    MFMA(o4_0, w13_4, h13_0); SCHED();
    MFMA(o4_1, w13_4, h13_1); SCHED();
    const u32x4 w14_2 = LDSR(pW_1, 61440);
    MFMA(o5_0, w13_5, h13_0); SCHED();
    MFMA(o5_1, w13_5, h13_1); SCHED();
    const u32x4 w14_3 = LDSR(pW_1, 63488);
    DMA2(wsrc, 638976, 49152);
    MFMA(o6_0, w13_6, h13_0); SCHED();
    MFMA(o6_1, w13_6, h13_1); SCHED();
    const u32x4 w14_4 = LDSR(pW_2, 0);
    asm volatile("s_waitcnt vmcnt(13)" : "+v"(h14_0), "+v"(h14_1)::"memory");
    MFMA(o0_0, w14_0, h14_0); SCHED();
    MFMA(o0_1, w14_0, h14_1); SCHED();
    const u32x4 w14_5 = LDSR(pW_2, 2048);
    MFMA(o1_0, w14_1, h14_0); SCHED();
    MFMA(o1_1, w14_1, h14_1); SCHED();
    const u32x4 w14_6 = LDSR(pW_2, 4096);
    MFMA(o2_0, w14_2, h14_0); SCHED();
    MFMA(o2_1, w14_2, h14_1); SCHED();
    const u32x4 w15_6 = LDSR(pW_2, 6144);
    MFMA(o3_0, w14_3, h14_0); SCHED();
    MFMA(o3_1, w14_3, h14_1); SCHED();
    const u32x4 w16_6 = LDSR(pW_2, 8192);
    MFMA(o4_0, w14_4, h14_0); SCHED();
    MFMA(o4_1, w14_4, h14_1); SCHED();
    const u32x4 w17_6 = LDSR(pW_2, 10240);
    MFMA(o5_0, w14_5, h14_0); SCHED();
    MFMA(o5_1, w14_5, h14_1); SCHED();
    const u32x4 w15_0 = LDSR(pW_2, 12288);
    MFMA(o6_0, w14_6, h14_0); SCHED();
    MFMA(o6_1, w14_6, h14_1); SCHED();
    WAITV(16); BAR();
    u32 pE_0 = l16; OPQ(pE_0); pE_0 += lds0 + rg * 1024; OPQ(pE_0);
    const u32x4 w16_0 = LDSR(pW_2, 14336);
    asm volatile("s_waitcnt vmcnt(9)" : "+v"(h15_0), "+v"(h15_1)::"memory");
    MFMA(o6_0, w15_6, h15_0); SCHED();
    MFMA(o6_1, w15_6, h15_1); SCHED();
    const u32x4 w17_0 = LDSR(pW_0, 0);
    asm volatile("s_waitcnt vmcnt(7)" : "+v"(h16_0), "+v"(h16_1)::"memory");
    MFMA(o6_0, w16_6, h16_0); SCHED();
    MFMA(o6_1, w16_6, h16_1); SCHED();
    const u32x4 w15_1 = LDSR(pW_0, 2048);
    asm volatile("s_waitcnt vmcnt(2)" : "+v"(fl5)::"memory"); CHECK(fl5, 1u, sFQ, 4);
    asm volatile("s_waitcnt vmcnt(3)" : "+v"(h17_0), "+v"(h17_1)::"memory");
    MFMA(o6_0, w17_6, h17_0); SCHED();
    MFMA(o6_1, w17_6, h17_1); SCHED();
    const u32x4 w16_1 = LDSR(pW_0, 4096);
    MFMA(o0_0, w15_0, h15_0); SCHED();
    MFMA(o0_1, w15_0, h15_1); SCHED();
    const u32x4 w17_1 = LDSR(pW_0, 6144);
    MFMA(o0_0, w16_0, h16_0); SCHED();
    WIN(pE, 1);
    MFMA(o0_1, w16_0, h16_1); SCHED();
    const u32x4 w15_2 = LDSR(pW_0, 8192);
    LDSWF(pE_1, 8192, o6_1);
    u32x4 gb0, gb1, gb2; GBLOAD();
    MFMA(o0_0, w17_0, h17_0); SCHED();
    MFMA(o0_1, w17_0, h17_1); SCHED();
    const u32x4 w16_2 = LDSR(pW_0, 10240);
    MFMA(o1_0, w15_1, h15_0); SCHED();
    MFMA(o1_1, w15_1, h15_1); SCHED();
    WAITV(14); WAITL0(); BAR();
    const u32x4 w17_2 = LDSR(pW_0, 12288);
    const f32x4 xo6 = LDSRF(pE_1, 12288);
    MFMA(o1_0, w16_1, h16_0); SCHED();
    MFMA(o1_1, w16_1, h16_1); SCHED();
    const u32x4 w15_3 = LDSR(pW_0, 14336);
    DMA2(wsrc, 655360, 65536);
    MFMA(o1_0, w17_1, h17_0); SCHED();
    MFMA(o1_1, w17_1, h17_1); SCHED();
    const u32x4 w16_3 = LDSR(pW_0, 16384);
    XADD(6, 0);
    XADD(6, 1);
    MFMA(o2_0, w15_2, h15_0); SCHED();
    XADD(6, 2);
    XADD(6, 3);
    MFMA(o2_1, w15_2, h15_1); SCHED();
    const u32x4 w17_3 = LDSR(pW_0, 18432);
    u32 pp6_0, pp6_1; XPACK(6);
    MFMA(o2_0, w16_2, h16_0); SCHED();
    MFMA(o2_1, w16_2, h16_1); SCHED();
    const u32x4 w15_4 = LDSR(pW_0, 20480);
    LDSWF(pE_1, 16384, o0_1);
    XST8();
    MFMA(o2_0, w17_2, h17_0); SCHED();
    LDSWF(pE_1, 24576, o1_1);
    MFMA(o2_1, w17_2, h17_1); SCHED();
    const u32x4 w16_4 = LDSR(pW_0, 22528);
    MFMA(o3_0, w15_3, h15_0); SCHED();
    MFMA(o3_1, w15_3, h15_1); SCHED();
    WAITV(11); WAITL0(); BAR();
    const u32x4 w17_4 = LDSR(pW_0, 24576);
    const f32x4 xo0 = LDSRF(pE_1, 20480);
    MFMA(o3_0, w16_3, h16_0); SCHED();
    const f32x4 xo1 = LDSRF(pE_1, 28672);
    MFMA(o3_1, w16_3, h16_1); SCHED();
    const u32x4 w15_5 = LDSR(pW_0, 26624);
    MFMA(o3_0, w17_3, h17_0); SCHED();
    MFMA(o3_1, w17_3, h17_1); SCHED();
    const u32x4 w16_5 = LDSR(pW_0, 28672);
    XADD(0, 0);
    XADD(0, 1);
    MFMA(o4_0, w15_4, h15_0); SCHED();
    XADD(0, 2);
    XADD(0, 3);
    MFMA(o4_1, w15_4, h15_1); SCHED();
    const u32x4 w17_5 = LDSR(pW_0, 30720);
    XADD(1, 0);
    XADD(1, 1);
    MFMA(o4_0, w16_4, h16_0); SCHED();
    XADD(1, 2);
    XADD(1, 3);
    MFMA(o4_1, w16_4, h16_1); SCHED();
    const u32x4 w8_7 = LDSR(pW_0, 32768);
    LDSWF(pE_1, 32768, o2_1);
    u32 pp0_0, pp0_1; XPACK(0);
    MFMA(o4_0, w17_4, h17_0); SCHED();
    LDSWF(pE_1, 40960, o3_1);
    MFMA(o4_1, w17_4, h17_1); SCHED();
    const u32x4 w8_8 = LDSR(pW_0, 34816);
    u32 pp1_0, pp1_1; XPACK(1);
    MFMA(o5_0, w15_5, h15_0); SCHED();
    MFMA(o5_1, w15_5, h15_1); SCHED();
    WAITV(11); WAITL0(); BAR();
    const u32x4 w9_7 = LDSR(pW_0, 36864);
    const f32x4 xo2 = LDSRF(pE_1, 36864);
    XST(0, 0, 1);
    MFMA(o5_0, w16_5, h16_0); SCHED();
    const f32x4 xo3 = LDSRF(pE_1, 45056);
    MFMA(o5_1, w16_5, h16_1); SCHED();
    const u32x4 w9_8 = LDSR(pW_0, 38912);
    MFMA(o5_0, w17_5, h17_0); SCHED();
    MFMA(o5_1, w17_5, h17_1); SCHED();
    const u32x4 w10_7 = LDSR(pW_0, 40960);
    XADD(2, 0);
    XADD(2, 1);
    MFMA(o7_0, w8_7, h8_0); SCHED();
    XADD(2, 2);
    XADD(2, 3);
    MFMA(o7_1, w8_7, h8_1); SCHED();
    const u32x4 w10_8 = LDSR(pW_0, 43008);
    XADD(3, 0);
    XADD(3, 1);
    MFMA(o8_0, w8_8, h8_0); SCHED();
    XADD(3, 2);
    XADD(3, 3);
    MFMA(o8_1, w8_8, h8_1); SCHED();
    const u32x4 w11_7 = LDSR(pW_0, 45056);
    LDSWF(pE_1, 49152, o4_1);
    u32 pp2_0, pp2_1; XPACK(2);
    MFMA(o7_0, w9_7, h9_0); SCHED();
    LDSWF(pE_1, 57344, o5_1);
    MFMA(o7_1, w9_7, h9_1); SCHED();
    const u32x4 w11_8 = LDSR(pW_0, 47104);
    u32 pp3_0, pp3_1; XPACK(3);
    MFMA(o8_0, w9_8, h9_0); SCHED();
    MFMA(o8_1, w9_8, h9_1); SCHED();
    WAITV(2); WAITL0(); BAR();
    const u32x4 w12_7 = LDSR(pW_0, 49152);
    const f32x4 xo4 = LDSRF(pE_1, 53248);
    XST(1, 2, 3);
    MFMA(o7_0, w10_7, h10_0); SCHED();
    const f32x4 xo5 = LDSRF(pE_1, 61440);
    MFMA(o7_1, w10_7, h10_1); SCHED();
    const u32x4 w12_8 = LDSR(pW_0, 51200);
    MFMA(o8_0, w10_8, h10_0); SCHED();
    MFMA(o8_1, w10_8, h10_1); SCHED();
    const u32x4 w13_7 = LDSR(pW_0, 53248);
    XADD(4, 0);
    XADD(4, 1);
    MFMA(o7_0, w11_7, h11_0); SCHED();
    XADD(4, 2);
    XADD(4, 3);
    MFMA(o7_1, w11_7, h11_1); SCHED();
    const u32x4 w13_8 = LDSR(pW_0, 55296);
    XADD(5, 0);
    XADD(5, 1);
    MFMA(o8_0, w11_8, h11_0); SCHED();
    XADD(5, 2);
    XADD(5, 3);
    MFMA(o8_1, w11_8, h11_1); SCHED();
    const u32x4 w14_7 = LDSR(pW_0, 57344);
    u32 pp4_0, pp4_1; XPACK(4);
    MFMA(o7_0, w12_7, h12_0); SCHED();
    MFMA(o7_1, w12_7, h12_1); SCHED();
    const u32x4 w14_8 = LDSR(pW_0, 59392);
    u32 pp5_0, pp5_1; XPACK(5);
    MFMA(o8_0, w12_8, h12_0); SCHED();
    MFMA(o8_1, w12_8, h12_1); SCHED();
    const u32x4 w15_7 = LDSR(pW_0, 61440);
    XST(2, 4, 5);
    MFMA(o7_0, w13_7, h13_0); SCHED();
    MFMA(o7_1, w13_7, h13_1); SCHED();
    const u32x4 w15_8 = LDSR(pW_0, 63488);
    MFMA(o8_0, w13_8, h13_0); SCHED();
    MFMA(o8_1, w13_8, h13_1); SCHED();
    const u32x4 w16_7 = LDSR(pW_1, 0);
    MFMA(o7_0, w14_7, h14_0); SCHED();
    MFMA(o7_1, w14_7, h14_1); SCHED();
    const u32x4 w16_8 = LDSR(pW_1, 2048);
    MFMA(o8_0, w14_8, h14_0); SCHED();
    MFMA(o8_1, w14_8, h14_1); SCHED();
    WAITV(4); BAR();
    const u32x4 w17_7 = LDSR(pW_1, 4096);
    MFMA(o7_0, w15_7, h15_0); SCHED();
    MFMA(o7_1, w15_7, h15_1); SCHED();
    const u32x4 w17_8 = LDSR(pW_1, 6144);
    u32x4 r0, r1, r2, r3, r4, r5; PLOAD();
    MFMA(o8_0, w15_8, h15_0); SCHED();
    MFMA(o8_1, w15_8, h15_1); SCHED();
    MFMA(o7_0, w16_7, h16_0); SCHED();
    MFMA(o7_1, w16_7, h16_1); SCHED();
    MFMA(o8_0, w16_8, h16_0); SCHED();
    MFMA(o8_1, w16_8, h16_1); SCHED();
    MFMA(o7_0, w17_7, h17_0); SCHED();
    MFMA(o7_1, w17_7, h17_1); SCHED();
    MFMA(o8_0, w17_8, h17_0); SCHED();
    MFMA(o8_1, w17_8, h17_1); SCHED();
    TAIL(2, 131072, 139264, 135168, 143360);
  } else {
    DMA2(wsrc, 0, 8192);
    DMA2(wsrc, 8192, 24576);
    DMA2(wsrc, 16384, 40960);
    DMA2(wsrc, 24576, 57344);
    SETUP();
    WAITV(4); BAR();
    const u32x2 xl0_0 = TRR(pX0_0, 0), xh0_0 = TRR(pX0_0, 4096); const u32x4 X0_0 = {xl0_0[0], xl0_0[1], xh0_0[0], xh0_0[1]};
    const u32x2 xl1_0 = TRR(pX1_0, 0), xh1_0 = TRR(pX1_0, 4096); const u32x4 X1_0 = {xl1_0[0], xl1_0[1], xh1_0[0], xh1_0[1]};
    const u32x2 xl0_1 = TRR(pX0_0, 16384), xh0_1 = TRR(pX0_0, 20480); const u32x4 X0_1 = {xl0_1[0], xl0_1[1], xh0_1[0], xh0_1[1]};
    const u32x4 a0_0_0 = LDSR(pW_0, 12288);
    const u32x4 a0_0_1 = LDSR(pW_0, 13312);
    const u32x4 a1_0_0 = LDSR(pW_0, 14336);
    const u32x4 a1_0_1 = LDSR(pW_0, 15360);
    f32x4 c0_0_0; MFMA0(c0_0_0, a0_0_0, X0_0); SCHED();
    const u32x2 xl1_1 = TRR(pX1_0, 16384), xh1_1 = TRR(pX1_0, 20480); const u32x4 X1_1 = {xl1_1[0], xl1_1[1], xh1_1[0], xh1_1[1]};
    f32x4 c0_0_1; MFMA0(c0_0_1, a0_0_0, X1_0); SCHED();
    const u32x4 a0_1_0 = LDSR(pW_0, 28672);
    f32x4 c0_1_0; MFMA0(c0_1_0, a0_0_1, X0_0); SCHED();
    DMA2(wsrc, 32768, 73728);
    f32x4 c0_1_1; MFMA0(c0_1_1, a0_0_1, X1_0); SCHED();
    const u32x4 a0_1_1 = LDSR(pW_0, 29696);
    f32x4 c1_0_0; MFMA0(c1_0_0, a1_0_0, X0_0); SCHED();
    f32x4 c1_0_1; MFMA0(c1_0_1, a1_0_0, X1_0); SCHED();
    const u32x4 a1_1_0 = LDSR(pW_0, 30720);
    f32x4 c1_1_0; MFMA0(c1_1_0, a1_0_1, X0_0); SCHED();
    DMA2(wsrc, 40960, 90112);
    f32x4 c1_1_1; MFMA0(c1_1_1, a1_0_1, X1_0); SCHED();
    const u32x4 a1_1_1 = LDSR(pW_0, 31744);
    f32x4 stS; MFMA0(stS, ones4, X0_0); SCHED();
    f32x4 stQ; MFMA0(stQ, X0_0, X0_0); SCHED();
    WAITV(6); WAITL0(); BAR();
    const u32x2 xl0_2 = TRR(pX0_0, 32768), xh0_2 = TRR(pX0_0, 36864); const u32x4 X0_2 = {xl0_2[0], xl0_2[1], xh0_2[0], xh0_2[1]};
    MFMA(c0_0_0, a0_1_0, X0_1); SCHED();
    const u32x2 xl1_2 = TRR(pX1_0, 32768), xh1_2 = TRR(pX1_0, 36864); const u32x4 X1_2 = {xl1_2[0], xl1_2[1], xh1_2[0], xh1_2[1]};
    MFMA(c0_0_1, a0_1_0, X1_1); SCHED();
    const u32x4 a0_2_0 = LDSR(pW_0, 45056);
    MFMA(c0_1_0, a0_1_1, X0_1); SCHED();
    DMA2(wsrc, 49152, 106496);
    MFMA(c0_1_1, a0_1_1, X1_1); SCHED();
    const u32x4 a0_2_1 = LDSR(pW_0, 46080);
    MFMA(c1_0_0, a1_1_0, X0_1); SCHED();
    MFMA(c1_0_1, a1_1_0, X1_1); SCHED();
    const u32x4 a1_2_0 = LDSR(pW_0, 47104);
    MFMA(c1_1_0, a1_1_1, X0_1); SCHED();
    DMA2(wsrc, 57344, 122880);
    MFMA(c1_1_1, a1_1_1, X1_1); SCHED();
    const u32x4 a1_2_1 = LDSR(pW_0, 48128);
    MFMA(stS, ones4, X0_1); SCHED();
    MFMA(stQ, X0_1, X0_1); SCHED();
    WAITV(8); BAR();
    const u32x2 xl0_3 = TRR(pX0_0, 49152), xh0_3 = TRR(pX0_0, 53248); const u32x4 X0_3 = {xl0_3[0], xl0_3[1], xh0_3[0], xh0_3[1]};
    MFMA(c0_0_0, a0_2_0, X0_2); SCHED();
    const u32x2 xl1_3 = TRR(pX1_0, 49152), xh1_3 = TRR(pX1_0, 53248); const u32x4 X1_3 = {xl1_3[0], xl1_3[1], xh1_3[0], xh1_3[1]};
    MFMA(c0_0_1, a0_2_0, X1_2); SCHED();
    const u32x4 a0_3_0 = LDSR(pW_0, 61440);
    MFMA(c0_1_0, a0_2_1, X0_2); SCHED();
    MFMA(c0_1_1, a0_2_1, X1_2); SCHED();
    const u32x4 a0_3_1 = LDSR(pW_0, 62464);
    MFMA(c1_0_0, a1_2_0, X0_2); SCHED();
    DMA2(wsrc, 65536, 139264);
    MFMA(c1_0_1, a1_2_0, X1_2); SCHED();
    const u32x4 a1_3_0 = LDSR(pW_0, 63488);
    MFMA(c1_1_0, a1_2_1, X0_2); SCHED();
    MFMA(c1_1_1, a1_2_1, X1_2); SCHED();
    const u32x4 a1_3_1 = LDSR(pW_0, 64512);
    MFMA(stS, ones4, X0_2); SCHED();
    MFMA(stQ, X0_2, X0_2); SCHED();
    WAITV(8); BAR();
    WIN(pX0, 1);
    const u32x2 xl0_4 = TRR(pX0_1, 0), xh0_4 = TRR(pX0_1, 4096); const u32x4 X0_4 = {xl0_4[0], xl0_4[1], xh0_4[0], xh0_4[1]};
    MFMA(c0_0_0, a0_3_0, X0_3); SCHED();
    WIN(pX1, 1);
    const u32x2 xl1_4 = TRR(pX1_1, 0), xh1_4 = TRR(pX1_1, 4096); const u32x4 X1_4 = {xl1_4[0], xl1_4[1], xh1_4[0], xh1_4[1]};
    MFMA(c0_0_1, a0_3_0, X1_3); SCHED();
    WIN(pW, 1);
    const u32x4 a0_4_0 = LDSR(pW_1, 12288);
    MFMA(c0_1_0, a0_3_1, X0_3); SCHED();
    MFMA(c0_1_1, a0_3_1, X1_3); SCHED();
    const u32x4 a0_4_1 = LDSR(pW_1, 13312);
    MFMA(c1_0_0, a1_3_0, X0_3); SCHED();
    DMA2(wsrc, 73728, 8192);
    MFMA(c1_0_1, a1_3_0, X1_3); SCHED();
    const u32x4 a1_4_0 = LDSR(pW_1, 14336);
    MFMA(c1_1_0, a1_3_1, X0_3); SCHED();
    MFMA(c1_1_1, a1_3_1, X1_3); SCHED();
    const u32x4 a1_4_1 = LDSR(pW_1, 15360);
    MFMA(stS, ones4, X0_3); SCHED();
    MFMA(stQ, X0_3, X0_3); SCHED();
    WAITV(8); BAR();
    const u32x2 xl0_5 = TRR(pX0_1, 16384), xh0_5 = TRR(pX0_1, 20480); const u32x4 X0_5 = {xl0_5[0], xl0_5[1], xh0_5[0], xh0_5[1]};
    MFMA(c0_0_0, a0_4_0, X0_4); SCHED();
    const u32x2 xl1_5 = TRR(pX1_1, 16384), xh1_5 = TRR(pX1_1, 20480); const u32x4 X1_5 = {xl1_5[0], xl1_5[1], xh1_5[0], xh1_5[1]};
    MFMA(c0_0_1, a0_4_0, X1_4); SCHED();
    const u32x4 a0_5_0 = LDSR(pW_1, 28672);
    MFMA(c0_1_0, a0_4_1, X0_4); SCHED();
    MFMA(c0_1_1, a0_4_1, X1_4); SCHED();
    const u32x4 a0_5_1 = LDSR(pW_1, 29696);
    MFMA(c1_0_0, a1_4_0, X0_4); SCHED();
    DMA2(wsrc, 81920, 24576);
    MFMA(c1_0_1, a1_4_0, X1_4); SCHED();
    const u32x4 a1_5_0 = LDSR(pW_1, 30720);
    MFMA(c1_1_0, a1_4_1, X0_4); SCHED();
    MFMA(c1_1_1, a1_4_1, X1_4); SCHED();
    const u32x4 a1_5_1 = LDSR(pW_1, 31744);
    MFMA(stS, ones4, X0_4); SCHED();
    MFMA(stQ, X0_4, X0_4); SCHED();
    WAITV(8); BAR();
    const u32x2 xl0_6 = TRR(pX0_1, 32768), xh0_6 = TRR(pX0_1, 36864); const u32x4 X0_6 = {xl0_6[0], xl0_6[1], xh0_6[0], xh0_6[1]};
    MFMA(c0_0_0, a0_5_0, X0_5); SCHED();
    const u32x2 xl1_6 = TRR(pX1_1, 32768), xh1_6 = TRR(pX1_1, 36864); const u32x4 X1_6 = {xl1_6[0], xl1_6[1], xh1_6[0], xh1_6[1]};
    MFMA(c0_0_1, a0_5_0, X1_5); SCHED();
    const u32x4 a0_6_0 = LDSR(pW_1, 45056);
    MFMA(c0_1_0, a0_5_1, X0_5); SCHED();
    MFMA(c0_1_1, a0_5_1, X1_5); SCHED();
    const u32x4 a0_6_1 = LDSR(pW_1, 46080);
    MFMA(c1_0_0, a1_5_0, X0_5); SCHED();
    DMA2(wsrc, 90112, 40960);
    MFMA(c1_0_1, a1_5_0, X1_5); SCHED();
    const u32x4 a1_6_0 = LDSR(pW_1, 47104);
    MFMA(c1_1_0, a1_5_1, X0_5); SCHED();
    MFMA(c1_1_1, a1_5_1, X1_5); SCHED();
    const u32x4 a1_6_1 = LDSR(pW_1, 48128);
    MFMA(stS, ones4, X0_5); SCHED();
    MFMA(stQ, X0_5, X0_5); SCHED();
    WAITV(8); BAR();
    const u32x2 xl0_7 = TRR(pX0_1, 49152), xh0_7 = TRR(pX0_1, 53248); const u32x4 X0_7 = {xl0_7[0], xl0_7[1], xh0_7[0], xh0_7[1]};
    MFMA(c0_0_0, a0_6_0, X0_6); SCHED();
    const u32x2 xl1_7 = TRR(pX1_1, 49152), xh1_7 = TRR(pX1_1, 53248); const u32x4 X1_7 = {xl1_7[0], xl1_7[1], xh1_7[0], xh1_7[1]};
    MFMA(c0_0_1, a0_6_0, X1_6); SCHED();
    const u32x4 a0_7_0 = LDSR(pW_1, 61440);
    MFMA(c0_1_0, a0_6_1, X0_6); SCHED();
    MFMA(c0_1_1, a0_6_1, X1_6); SCHED();
    const u32x4 a0_7_1 = LDSR(pW_1, 62464);
    MFMA(c1_0_0, a1_6_0, X0_6); SCHED();
    DMA2(wsrc, 98304, 57344);
    MFMA(c1_0_1, a1_6_0, X1_6); SCHED();
    const u32x4 a1_7_0 = LDSR(pW_1, 63488);
    MFMA(c1_1_0, a1_6_1, X0_6); SCHED();
    MFMA(c1_1_1, a1_6_1, X1_6); SCHED();
    const u32x4 a1_7_1 = LDSR(pW_1, 64512);
    MFMA(stS, ones4, X0_6); SCHED();
    MFMA(stQ, X0_6, X0_6); SCHED();
    WAITV(8); BAR();
    WIN(pX0, 2);
    const u32x2 xl0_8 = TRR(pX0_2, 0), xh0_8 = TRR(pX0_2, 4096); const u32x4 X0_8 = {xl0_8[0], xl0_8[1], xh0_8[0], xh0_8[1]};
    MFMA(c0_0_0, a0_7_0, X0_7); SCHED();
    WIN(pX1, 2);
    const u32x2 xl1_8 = TRR(pX1_2, 0), xh1_8 = TRR(pX1_2, 4096); const u32x4 X1_8 = {xl1_8[0], xl1_8[1], xh1_8[0], xh1_8[1]};
    MFMA(c0_0_1, a0_7_0, X1_7); SCHED();
    WIN(pW, 2);
    const u32x4 a0_8_0 = LDSR(pW_2, 12288);
    MFMA(c0_1_0, a0_7_1, X0_7); SCHED();
    MFMA(c0_1_1, a0_7_1, X1_7); SCHED();
    const u32x4 a0_8_1 = LDSR(pW_2, 13312);
    MFMA(c1_0_0, a1_7_0, X0_7); SCHED();
    DMA2(wsrc, 106496, 73728);
    MFMA(c1_0_1, a1_7_0, X1_7); SCHED();
    const u32x4 a1_8_0 = LDSR(pW_2, 14336);
    MFMA(c1_1_0, a1_7_1, X0_7); SCHED();
    MFMA(c1_1_1, a1_7_1, X1_7); SCHED();
    const u32x4 a1_8_1 = LDSR(pW_2, 15360);
    MFMA(stS, ones4, X0_7); SCHED();
    MFMA(stQ, X0_7, X0_7); SCHED();
    WAITV(8); BAR();
    const u32x2 xl0_9 = TRR(pX0_0, 0), xh0_9 = TRR(pX0_0, 4096); const u32x4 X0_9 = {xl0_9[0], xl0_9[1], xh0_9[0], xh0_9[1]};
    MFMA(c0_0_0, a0_8_0, X0_8); SCHED();
    const u32x2 xl1_9 = TRR(pX1_0, 0), xh1_9 = TRR(pX1_0, 4096); const u32x4 X1_9 = {xl1_9[0], xl1_9[1], xh1_9[0], xh1_9[1]};
    MFMA(c0_0_1, a0_8_0, X1_8); SCHED();
    const u32x4 a0_9_0 = LDSR(pW_0, 12288);
    MFMA(c0_1_0, a0_8_1, X0_8); SCHED();
    MFMA(c0_1_1, a0_8_1, X1_8); SCHED();
    const u32x4 a0_9_1 = LDSR(pW_0, 13312);
    MFMA(c1_0_0, a1_8_0, X0_8); SCHED();
    DMA2(wsrc, 114688, 90112);
    MFMA(c1_0_1, a1_8_0, X1_8); SCHED();
    const u32x4 a1_9_0 = LDSR(pW_0, 14336);
    MFMA(c1_1_0, a1_8_1, X0_8); SCHED();
    MFMA(c1_1_1, a1_8_1, X1_8); SCHED();
    const u32x4 a1_9_1 = LDSR(pW_0, 15360);
    MFMA(stS, ones4, X0_8); SCHED();
    MFMA(stQ, X0_8, X0_8); SCHED();
    WAITV(8); BAR();
    const u32x2 xl0_10 = TRR(pX0_0, 16384), xh0_10 = TRR(pX0_0, 20480); const u32x4 X0_10 = {xl0_10[0], xl0_10[1], xh0_10[0], xh0_10[1]};
    MFMA(c0_0_0, a0_9_0, X0_9); SCHED();
    const u32x2 xl1_10 = TRR(pX1_0, 16384), xh1_10 = TRR(pX1_0, 20480); const u32x4 X1_10 = {xl1_10[0], xl1_10[1], xh1_10[0], xh1_10[1]};
    MFMA(c0_0_1, a0_9_0, X1_9); SCHED();
    const u32x4 a0_10_0 = LDSR(pW_0, 28672);
    MFMA(c0_1_0, a0_9_1, X0_9); SCHED();
    MFMA(c0_1_1, a0_9_1, X1_9); SCHED();
    const u32x4 a0_10_1 = LDSR(pW_0, 29696);
    MFMA(c1_0_0, a1_9_0, X0_9); SCHED();
    DMA2(wsrc, 122880, 106496);
    MFMA(c1_0_1, a1_9_0, X1_9); SCHED();
    const u32x4 a1_10_0 = LDSR(pW_0, 30720);
    MFMA(c1_1_0, a1_9_1, X0_9); SCHED();
    MFMA(c1_1_1, a1_9_1, X1_9); SCHED();
    const u32x4 a1_10_1 = LDSR(pW_0, 31744);
    MFMA(stS, ones4, X0_9); SCHED();
    MFMA(stQ, X0_9, X0_9); SCHED();
    WAITV(8); BAR();
    const u32x2 xl0_11 = TRR(pX0_0, 32768), xh0_11 = TRR(pX0_0, 36864); const u32x4 X0_11 = {xl0_11[0], xl0_11[1], xh0_11[0], xh0_11[1]};
    MFMA(c0_0_0, a0_10_0, X0_10); SCHED();
    const u32x2 xl1_11 = TRR(pX1_0, 32768), xh1_11 = TRR(pX1_0, 36864); const u32x4 X1_11 = {xl1_11[0], xl1_11[1], xh1_11[0], xh1_11[1]};
    MFMA(c0_0_1, a0_10_0, X1_10); SCHED();
    const u32x4 a0_11_0 = LDSR(pW_0, 45056);
    MFMA(c0_1_0, a0_10_1, X0_10); SCHED();
    MFMA(c0_1_1, a0_10_1, X1_10); SCHED();
    const u32x4 a0_11_1 = LDSR(pW_0, 46080);
    MFMA(c1_0_0, a1_10_0, X0_10); SCHED();
    DMA2(wsrc, 131072, 122880);
    MFMA(c1_0_1, a1_10_0, X1_10); SCHED();
    const u32x4 a1_11_0 = LDSR(pW_0, 47104);
    MFMA(c1_1_0, a1_10_1, X0_10); SCHED();
    MFMA(c1_1_1, a1_10_1, X1_10); SCHED();
    const u32x4 a1_11_1 = LDSR(pW_0, 48128);
    MFMA(stS, ones4, X0_10); SCHED();
    MFMA(stQ, X0_10, X0_10); SCHED();
    WAITV(8); BAR();
    const u32x2 xl0_12 = TRR(pX0_0, 49152), xh0_12 = TRR(pX0_0, 53248); const u32x4 X0_12 = {xl0_12[0], xl0_12[1], xh0_12[0], xh0_12[1]};
    MFMA(c0_0_0, a0_11_0, X0_11); SCHED();
    const u32x2 xl1_12 = TRR(pX1_0, 49152), xh1_12 = TRR(pX1_0, 53248); const u32x4 X1_12 = {xl1_12[0], xl1_12[1], xh1_12[0], xh1_12[1]};
    MFMA(c0_0_1, a0_11_0, X1_11); SCHED();
    const u32x4 a0_12_0 = LDSR(pW_0, 61440);
    MFMA(c0_1_0, a0_11_1, X0_11); SCHED();
    MFMA(c0_1_1, a0_11_1, X1_11); SCHED();
    const u32x4 a0_12_1 = LDSR(pW_0, 62464);
    MFMA(c1_0_0, a1_11_0, X0_11); SCHED();
    DMA2(wsrc, 139264, 139264);
    MFMA(c1_0_1, a1_11_0, X1_11); SCHED();
    const u32x4 a1_12_0 = LDSR(pW_0, 63488);
    MFMA(c1_1_0, a1_11_1, X0_11); SCHED();
    MFMA(c1_1_1, a1_11_1, X1_11); SCHED();
    const u32x4 a1_12_1 = LDSR(pW_0, 64512);
    MFMA(stS, ones4, X0_11); SCHED();
    MFMA(stQ, X0_11, X0_11); SCHED();
    WAITV(8); BAR();
    const u32x2 xl0_13 = TRR(pX0_1, 0), xh0_13 = TRR(pX0_1, 4096); const u32x4 X0_13 = {xl0_13[0], xl0_13[1], xh0_13[0], xh0_13[1]};
    MFMA(c0_0_0, a0_12_0, X0_12); SCHED();
    const u32x2 xl1_13 = TRR(pX1_1, 0), xh1_13 = TRR(pX1_1, 4096); const u32x4 X1_13 = {xl1_13[0], xl1_13[1], xh1_13[0], xh1_13[1]};
    MFMA(c0_0_1, a0_12_0, X1_12); SCHED();
    const u32x4 a0_13_0 = LDSR(pW_1, 12288);
    MFMA(c0_1_0, a0_12_1, X0_12); SCHED();
    MFMA(c0_1_1, a0_12_1, X1_12); SCHED();
    const u32x4 a0_13_1 = LDSR(pW_1, 13312);
    MFMA(c1_0_0, a1_12_0, X0_12); SCHED();
    DMA2(wsrc, 155648, 8192);
    MFMA(c1_0_1, a1_12_0, X1_12); SCHED();
    const u32x4 a1_13_0 = LDSR(pW_1, 14336);
    MFMA(c1_1_0, a1_12_1, X0_12); SCHED();
    MFMA(c1_1_1, a1_12_1, X1_12); SCHED();
    const u32x4 a1_13_1 = LDSR(pW_1, 15360);
    MFMA(stS, ones4, X0_12); SCHED();
    MFMA(stQ, X0_12, X0_12); SCHED();
    WAITV(8); BAR();
    const u32x2 xl0_14 = TRR(pX0_1, 16384), xh0_14 = TRR(pX0_1, 20480); const u32x4 X0_14 = {xl0_14[0], xl0_14[1], xh0_14[0], xh0_14[1]};
    MFMA(c0_0_0, a0_13_0, X0_13); SCHED();
    const u32x2 xl1_14 = TRR(pX1_1, 16384), xh1_14 = TRR(pX1_1, 20480); const u32x4 X1_14 = {xl1_14[0], xl1_14[1], xh1_14[0], xh1_14[1]};
    MFMA(c0_0_1, a0_13_0, X1_13); SCHED();
    const u32x4 a0_14_0 = LDSR(pW_1, 28672);
    MFMA(c0_1_0, a0_13_1, X0_13); SCHED();
    MFMA(c0_1_1, a0_13_1, X1_13); SCHED();
    const u32x4 a0_14_1 = LDSR(pW_1, 29696);
    MFMA(c1_0_0, a1_13_0, X0_13); SCHED();
    DMA2(wsrc, 172032, 24576);
    MFMA(c1_0_1, a1_13_0, X1_13); SCHED();
    const u32x4 a1_14_0 = LDSR(pW_1, 30720);
    MFMA(c1_1_0, a1_13_1, X0_13); SCHED();
    MFMA(c1_1_1, a1_13_1, X1_13); SCHED();
    const u32x4 a1_14_1 = LDSR(pW_1, 31744);
    MFMA(stS, ones4, X0_13); SCHED();
    MFMA(stQ, X0_13, X0_13); SCHED();
    WAITV(8); BAR();
    const u32x2 xl0_15 = TRR(pX0_1, 32768), xh0_15 = TRR(pX0_1, 36864); const u32x4 X0_15 = {xl0_15[0], xl0_15[1], xh0_15[0], xh0_15[1]};
    MFMA(c0_0_0, a0_14_0, X0_14); SCHED();
    const u32x2 xl1_15 = TRR(pX1_1, 32768), xh1_15 = TRR(pX1_1, 36864); const u32x4 X1_15 = {xl1_15[0], xl1_15[1], xh1_15[0], xh1_15[1]};
    MFMA(c0_0_1, a0_14_0, X1_14); SCHED();
    const u32x4 a0_15_0 = LDSR(pW_1, 45056);
    MFMA(c0_1_0, a0_14_1, X0_14); SCHED();
    MFMA(c0_1_1, a0_14_1, X1_14); SCHED();
    const u32x4 a0_15_1 = LDSR(pW_1, 46080);
    MFMA(c1_0_0, a1_14_0, X0_14); SCHED();
    DMA2(wsrc, 188416, 40960);
    MFMA(c1_0_1, a1_14_0, X1_14); SCHED();
    const u32x4 a1_15_0 = LDSR(pW_1, 47104);
    MFMA(c1_1_0, a1_14_1, X0_14); SCHED();
    MFMA(c1_1_1, a1_14_1, X1_14); SCHED();
    const u32x4 a1_15_1 = LDSR(pW_1, 48128);
    MFMA(stS, ones4, X0_14); SCHED();
    MFMA(stQ, X0_14, X0_14); SCHED();
    WAITV(8); BAR();
    const u32x2 xl0_16 = TRR(pX0_1, 49152), xh0_16 = TRR(pX0_1, 53248); const u32x4 X0_16 = {xl0_16[0], xl0_16[1], xh0_16[0], xh0_16[1]};
    MFMA(c0_0_0, a0_15_0, X0_15); SCHED();
    const u32x2 xl1_16 = TRR(pX1_1, 49152), xh1_16 = TRR(pX1_1, 53248); const u32x4 X1_16 = {xl1_16[0], xl1_16[1], xh1_16[0], xh1_16[1]};
    MFMA(c0_0_1, a0_15_0, X1_15); SCHED();
    const u32x4 a0_16_0 = LDSR(pW_1, 61440);
    MFMA(c0_1_0, a0_15_1, X0_15); SCHED();
    MFMA(c0_1_1, a0_15_1, X1_15); SCHED();
    const u32x4 a0_16_1 = LDSR(pW_1, 62464);
    MFMA(c1_0_0, a1_15_0, X0_15); SCHED();
    DMA2(wsrc, 204800, 57344);
    MFMA(c1_0_1, a1_15_0, X1_15); SCHED();
    const u32x4 a1_16_0 = LDSR(pW_1, 63488);
    MFMA(c1_1_0, a1_15_1, X0_15); SCHED();
    MFMA(c1_1_1, a1_15_1, X1_15); SCHED();
    const u32x4 a1_16_1 = LDSR(pW_1, 64512);
    MFMA(stS, ones4, X0_15); SCHED();
    MFMA(stQ, X0_15, X0_15); SCHED();
    WAITV(8); BAR();
    const u32x2 xl0_17 = TRR(pX0_2, 0), xh0_17 = TRR(pX0_2, 4096); const u32x4 X0_17 = {xl0_17[0], xl0_17[1], xh0_17[0], xh0_17[1]};
    MFMA(c0_0_0, a0_16_0, X0_16); SCHED();
    const u32x2 xl1_17 = TRR(pX1_2, 0), xh1_17 = TRR(pX1_2, 4096); const u32x4 X1_17 = {xl1_17[0], xl1_17[1], xh1_17[0], xh1_17[1]};
    MFMA(c0_0_1, a0_16_0, X1_16); SCHED();
    const u32x4 a0_17_0 = LDSR(pW_2, 12288);
    MFMA(c0_1_0, a0_16_1, X0_16); SCHED();
    MFMA(c0_1_1, a0_16_1, X1_16); SCHED();
    const u32x4 a0_17_1 = LDSR(pW_2, 13312);
    MFMA(c1_0_0, a1_16_0, X0_16); SCHED();
    DMA2(wsrc, 221184, 73728);
    MFMA(c1_0_1, a1_16_0, X1_16); SCHED();
    const u32x4 a1_17_0 = LDSR(pW_2, 14336);
    MFMA(c1_1_0, a1_16_1, X0_16); SCHED();
    MFMA(c1_1_1, a1_16_1, X1_16); SCHED();
    const u32x4 a1_17_1 = LDSR(pW_2, 15360);
    MFMA(stS, ones4, X0_16); SCHED();
    MFMA(stQ, X0_16, X0_16); SCHED();
    WAITV(10); BAR();
    MFMA(c0_0_0, a0_17_0, X0_17); SCHED();
    MFMA(c0_0_1, a0_17_0, X1_17); SCHED();
    const u32x4 a2_0_0 = LDSR(pW_0, 2048);
    MFMA(c0_1_0, a0_17_1, X0_17); SCHED();
    MFMA(c0_1_1, a0_17_1, X1_17); SCHED();
    const u32x4 a2_0_1 = LDSR(pW_0, 3072);
    MFMA(c1_0_0, a1_17_0, X0_17); SCHED();
    MFMA(c1_0_1, a1_17_0, X1_17); SCHED();
    const u32x4 a2_1_0 = LDSR(pW_0, 6144);
    MFMA(c1_1_0, a1_17_1, X0_17); SCHED();
    MFMA(c1_1_1, a1_17_1, X1_17); SCHED();
    const u32x4 a2_1_1 = LDSR(pW_0, 7168);
    MFMA(stS, ones4, X0_17); SCHED();
    MFMA(stQ, X0_17, X0_17); SCHED();
    WAITV(6); BAR();
    const u32x4 a2_2_0 = LDSR(pW_0, 10240);
    f32x4 c2_0_0; MFMA0(c2_0_0, a2_0_0, X0_0); SCHED();
    f32x4 c2_0_1; MFMA0(c2_0_1, a2_0_0, X1_0); SCHED();
    const u32x4 a2_2_1 = LDSR(pW_0, 11264);
    DMA2(wsrc, 237568, 90112);
    f32x4 c2_1_0; MFMA0(c2_1_0, a2_0_1, X0_0); SCHED();
    f32x4 c2_1_1; MFMA0(c2_1_1, a2_0_1, X1_0); SCHED();
    const u32x4 a2_3_0 = LDSR(pW_0, 14336);
    float qd_; STAT_DIAG(qd_); asm volatile("" :: "v"(stS));
    MFMA(c2_0_0, a2_1_0, X0_1); SCHED();
    STAT_BCAST(qd_);
    MFMA(c2_0_1, a2_1_0, X1_1); SCHED();
    const u32x4 a2_3_1 = LDSR(pW_0, 15360);
    float mean_; V_MULC(mean_, stS[0], 1.f / D_);
    MFMA(c2_1_0, a2_1_1, X0_1); SCHED();
    float m2_ = mean_ * mean_; OPQ(m2_); mean_ = rbf(mean_); OPQ(mean_);
    MFMA(c2_1_1, a2_1_1, X1_1); SCHED();
    const u32x4 a2_4_0 = LDSR(pW_0, 18432);
    MFMA(c2_0_0, a2_2_0, X0_2); SCHED();
    float var_ = fmaxf(__builtin_fmaf(qd_, 1.f / D_, -m2_), 0.f); OPQ(var_);
    MFMA(c2_0_1, a2_2_0, X1_2); SCHED();
    const u32x4 a2_4_1 = LDSR(pW_0, 19456);
    var_ = rbf(var_) + 1e-5f; OPQ(var_);
    PREFILL(0);
    MFMA(c2_1_0, a2_2_1, X0_2); SCHED();
    const f32x4 v1_r0_0_0 = LDSRF(pV, 128), v2_r0_0_0 = LDSRF(pV, 1280);
    var_ = __builtin_amdgcn_sqrtf(rbf(var_)); OPQ(var_);
    MFMA(c2_1_1, a2_2_1, X1_2); SCHED();
    const u32x4 a2_5_0 = LDSR(pW_0, 22528);
    var_ = __builtin_amdgcn_rcpf(rbf(var_)); OPQ(var_);
    MFMA(c2_0_0, a2_3_0, X0_3); SCHED();
    float R0 = var_ * (-LOG2E); OPQ(R0); float M0 = -(mean_ * R0); OPQ(M0);
    DMA2(wsrc, 253952, 106496);
    MFMA(c2_0_1, a2_3_0, X1_3); SCHED();
    const u32x4 a2_5_1 = LDSR(pW_0, 23552);
    { const f32x2 mr_ = {M0, R0}; LDSW2(pS, 0, mr_); }
    MFMA(c2_1_0, a2_3_1, X0_3); SCHED();
    float t0a; V_FMA(t0a, M0, v1_r0_0_0[0], v2_r0_0_0[0]);
    float t0b; V_FMA(t0b, M0, v1_r0_0_0[1], v2_r0_0_0[1]);
    MFMA(c2_1_1, a2_3_1, X1_3); SCHED();
    const u32x4 a2_6_0 = LDSR(pW_0, 26624);
    V_FMAC(t0a, c0_0_0[0], R0);
    V_FMAC(t0b, c0_0_0[1], R0);
    MFMA(c2_0_0, a2_4_0, X0_4); SCHED();
    float e0a; V_EXP(e0a, t0a);
    float e0b; V_EXP(e0b, t0b);
    MFMA(c2_0_1, a2_4_0, X1_4); SCHED();
    const u32x4 a2_6_1 = LDSR(pW_0, 27648);
    V_ADD1(e0a);
    V_ADD1(e0b);
    MFMA(c2_1_0, a2_4_1, X0_4); SCHED();
    V_RCP(e0a);
    V_RCP(e0b);
    PREFILL(1);
    MFMA(c2_1_1, a2_4_1, X1_4); SCHED();
    const u32x4 a2_7_0 = LDSR(pW_0, 30720);
    V_MUL(t0a, e0a);
    V_MUL(t0b, e0b);
    MFMA(c2_0_0, a2_5_0, X0_5); SCHED();
    u32 hp_r0_0_0; V_CVT(hp_r0_0_0, t0a, t0b);
    float t1a; V_FMA(t1a, M0, v1_r0_0_0[2], v2_r0_0_0[2]);
    MFMA(c2_0_1, a2_5_0, X1_5); SCHED();
    const f32x4 v1_r0_0_1 = LDSRF(pV, 192), v2_r0_0_1 = LDSRF(pV, 1344);
    const u32x4 a2_7_1 = LDSR(pW_0, 31744);
    float t1b; V_FMA(t1b, M0, v1_r0_0_0[3], v2_r0_0_0[3]);
    V_FMAC(t1a, c0_0_0[2], R0);
    MFMA(c2_1_0, a2_5_1, X0_5); SCHED();
    V_FMAC(t1b, c0_0_0[3], R0);
    float e1a; V_EXP(e1a, t1a);
    MFMA(c2_1_1, a2_5_1, X1_5); SCHED();
    WAITV(10); WAITL0(); BAR();
    const f32x2 mr1 = LDSR2(pS, -512); const float M1 = mr1[0], R1 = mr1[1];
    const u32x4 a2_8_0 = LDSR(pW_0, 34816);
    float e1b; V_EXP(e1b, t1b);
    V_ADD1(e1a);
    MFMA(c2_0_0, a2_6_0, X0_6); SCHED();
    V_ADD1(e1b);
    V_RCP(e1a);
    MFMA(c2_0_1, a2_6_0, X1_6); SCHED();
    const u32x4 a2_8_1 = LDSR(pW_0, 35840);
    V_RCP(e1b);
    V_MUL(t1a, e1a);
    DMA2(wsrc, 270336, 122880);
    MFMA(c2_1_0, a2_6_1, X0_6); SCHED();
    V_MUL(t1b, e1b);
    u32 hp_r0_0_1; V_CVT(hp_r0_0_1, t1a, t1b);
    MFMA(c2_1_1, a2_6_1, X1_6); SCHED();
    const u32x4 a2_9_0 = LDSR(pW_0, 38912);
    float t2a; V_FMA(t2a, M0, v1_r0_0_1[0], v2_r0_0_1[0]);
    PREFILL(2);
    MFMA(c2_0_0, a2_7_0, X0_7); SCHED();
    float t2b; V_FMA(t2b, M0, v1_r0_0_1[1], v2_r0_0_1[1]);
    V_FMAC(t2a, c0_1_0[0], R0);
    MFMA(c2_0_1, a2_7_0, X1_7); SCHED();
    const u32x4 a2_9_1 = LDSR(pW_0, 39936);
    V_FMAC(t2b, c0_1_0[1], R0);
    float e2a; V_EXP(e2a, t2a);
    MFMA(c2_1_0, a2_7_1, X0_7); SCHED();
    float e2b; V_EXP(e2b, t2b);
    V_ADD1(e2a);
    MFMA(c2_1_1, a2_7_1, X1_7); SCHED();
    const u32x4 a2_10_0 = LDSR(pW_0, 43008);
    V_ADD1(e2b);
    V_RCP(e2a);
    MFMA(c2_0_0, a2_8_0, X0_8); SCHED();
    V_RCP(e2b);
    V_MUL(t2a, e2a);
    MFMA(c2_0_1, a2_8_0, X1_8); SCHED();
    const u32x4 a2_10_1 = LDSR(pW_0, 44032);
    V_MUL(t2b, e2b);
    u32 hp_r0_0_2; V_CVT(hp_r0_0_2, t2a, t2b);
    MFMA(c2_1_0, a2_8_1, X0_8); SCHED();
    float t3a; V_FMA(t3a, M0, v1_r0_0_1[2], v2_r0_0_1[2]);
    float t3b; V_FMA(t3b, M0, v1_r0_0_1[3], v2_r0_0_1[3]);
    MFMA(c2_1_1, a2_8_1, X1_8); SCHED();
    const f32x4 v1_r0_1_0 = LDSRF(pV, 128), v2_r0_1_0 = LDSRF(pV, 1280);
    const u32x4 a2_11_0 = LDSR(pW_0, 47104);
    V_FMAC(t3a, c0_1_0[2], R0);
    V_FMAC(t3b, c0_1_0[3], R0);
    MFMA(c2_0_0, a2_9_0, X0_9); SCHED();
    float e3a; V_EXP(e3a, t3a);
    float e3b; V_EXP(e3b, t3b);
    DMA2(wsrc, 286720, 139264);
    PREFILL(3);
    MFMA(c2_0_1, a2_9_0, X1_9); SCHED();
    const u32x4 a2_11_1 = LDSR(pW_0, 48128);
    V_ADD1(e3a);
    V_ADD1(e3b);
    MFMA(c2_1_0, a2_9_1, X0_9); SCHED();
    V_RCP(e3a);
    V_RCP(e3b);
    MFMA(c2_1_1, a2_9_1, X1_9); SCHED();
    const u32x4 a2_12_0 = LDSR(pW_0, 51200);
    V_MUL(t3a, e3a);
    V_MUL(t3b, e3b);
    MFMA(c2_0_0, a2_10_0, X0_10); SCHED();
    u32 hp_r0_0_3; V_CVT(hp_r0_0_3, t3a, t3b);
    MFMA(c2_0_1, a2_10_0, X1_10); SCHED();
    const u32x4 a2_12_1 = LDSR(pW_0, 52224);
    float t4a; V_FMA(t4a, M1, v1_r0_1_0[0], v2_r0_1_0[0]);
    float t4b; V_FMA(t4b, M1, v1_r0_1_0[1], v2_r0_1_0[1]);
    MFMA(c2_1_0, a2_10_1, X0_10); SCHED();
    V_FMAC(t4a, c0_0_1[0], R1);
    V_FMAC(t4b, c0_0_1[1], R1);
    MFMA(c2_1_1, a2_10_1, X1_10); SCHED();
    const u32x4 a2_13_0 = LDSR(pW_0, 55296);
    float e4a; V_EXP(e4a, t4a);
    float e4b; V_EXP(e4b, t4b);
    const u32x4 hv_r0_0 = {hp_r0_0_0, hp_r0_0_1, hp_r0_0_2, hp_r0_0_3}; HST(sHS0 + 1024, hv_r0_0);
    MFMA(c2_0_0, a2_11_0, X0_11); SCHED();
    V_ADD1(e4a);
    V_ADD1(e4b);
    MFMA(c2_0_1, a2_11_0, X1_11); SCHED();
    const u32x4 a2_13_1 = LDSR(pW_0, 56320);
    V_RCP(e4a);
    V_RCP(e4b);
    MFMA(c2_1_0, a2_11_1, X0_11); SCHED();
    V_MUL(t4a, e4a);
    V_MUL(t4b, e4b);
    MFMA(c2_1_1, a2_11_1, X1_11); SCHED();
    WAITV(13); BAR();
    const u32x4 a2_14_0 = LDSR(pW_0, 59392);
    u32 hp_r0_1_0; V_CVT(hp_r0_1_0, t4a, t4b);
    float t5a; V_FMA(t5a, M1, v1_r0_1_0[2], v2_r0_1_0[2]);
    MFMA(c2_0_0, a2_12_0, X0_12); SCHED();
    const f32x4 v1_r0_1_1 = LDSRF(pV, 192), v2_r0_1_1 = LDSRF(pV, 1344);
    float t5b; V_FMA(t5b, M1, v1_r0_1_0[3], v2_r0_1_0[3]);
    V_FMAC(t5a, c0_0_1[2], R1);
    MFMA(c2_0_1, a2_12_0, X1_12); SCHED();
    const u32x4 a2_14_1 = LDSR(pW_0, 60416);
    V_FMAC(t5b, c0_0_1[3], R1);
    float e5a; V_EXP(e5a, t5a);
    DMA2(wsrc, 303104, 8192);
    MFMA(c2_1_0, a2_12_1, X0_12); SCHED();
    float e5b; V_EXP(e5b, t5b);
    V_ADD1(e5a);
    MFMA(c2_1_1, a2_12_1, X1_12); SCHED();
    const u32x4 a2_15_0 = LDSR(pW_0, 63488);
    V_ADD1(e5b);
    V_RCP(e5a);
    MFMA(c2_0_0, a2_13_0, X0_13); SCHED();
    V_RCP(e5b);
    V_MUL(t5a, e5a);
    MFMA(c2_0_1, a2_13_0, X1_13); SCHED();
    const u32x4 a2_15_1 = LDSR(pW_0, 64512);
    V_MUL(t5b, e5b);
    u32 hp_r0_1_1; V_CVT(hp_r0_1_1, t5a, t5b);
    MFMA(c2_1_0, a2_13_1, X0_13); SCHED();
    float t6a; V_FMA(t6a, M1, v1_r0_1_1[0], v2_r0_1_1[0]);
    MFMA(c2_1_1, a2_13_1, X1_13); SCHED();
    const u32x4 a2_16_0 = LDSR(pW_1, 2048);
    float t6b; V_FMA(t6b, M1, v1_r0_1_1[1], v2_r0_1_1[1]);
    V_FMAC(t6a, c0_1_1[0], R1);
    MFMA(c2_0_0, a2_14_0, X0_14); SCHED();
    V_FMAC(t6b, c0_1_1[1], R1);
    float e6a; V_EXP(e6a, t6a);
    MFMA(c2_0_1, a2_14_0, X1_14); SCHED();
    const u32x4 a2_16_1 = LDSR(pW_1, 3072);
    float e6b; V_EXP(e6b, t6b);
    V_ADD1(e6a);
    MFMA(c2_1_0, a2_14_1, X0_14); SCHED();
    V_ADD1(e6b);
    V_RCP(e6a);
    MFMA(c2_1_1, a2_14_1, X1_14); SCHED();
    const u32x4 a2_17_0 = LDSR(pW_1, 6144);
    V_RCP(e6b);
    V_MUL(t6a, e6a);
    MFMA(c2_0_0, a2_15_0, X0_15); SCHED();
    V_MUL(t6b, e6b);
    u32 hp_r0_1_2; V_CVT(hp_r0_1_2, t6a, t6b);
    DMA2(wsrc, 319488, 24576);
    MFMA(c2_0_1, a2_15_0, X1_15); SCHED();
    const u32x4 a2_17_1 = LDSR(pW_1, 7168);
    float t7a; V_FMA(t7a, M1, v1_r0_1_1[2], v2_r0_1_1[2]);
    float t7b; V_FMA(t7b, M1, v1_r0_1_1[3], v2_r0_1_1[3]);
    MFMA(c2_1_0, a2_15_1, X0_15); SCHED();
    V_FMAC(t7a, c0_1_1[2], R1);
    V_FMAC(t7b, c0_1_1[3], R1);
    MFMA(c2_1_1, a2_15_1, X1_15); SCHED();
    const u32x4 a3_0_0 = LDSR(pW_1, 10240);
    float e7a; V_EXP(e7a, t7a);
    float e7b; V_EXP(e7b, t7b);
    MFMA(c2_0_0, a2_16_0, X0_16); SCHED();
    V_ADD1(e7a);
    V_ADD1(e7b);
    MFMA(c2_0_1, a2_16_0, X1_16); SCHED();
    const u32x4 a3_0_1 = LDSR(pW_1, 11264);
    V_RCP(e7a);
    V_RCP(e7b);
    MFMA(c2_1_0, a2_16_1, X0_16); SCHED();
    const f32x4 v1_r1_0_0 = LDSRF(pV, 384), v2_r1_0_0 = LDSRF(pV, 1536);
    V_MUL(t7a, e7a);
    V_MUL(t7b, e7b);
    MFMA(c2_1_1, a2_16_1, X1_16); SCHED();
    const u32x4 a3_1_0 = LDSR(pW_1, 14336);
    u32 hp_r0_1_3; V_CVT(hp_r0_1_3, t7a, t7b);
    MFMA(c2_0_0, a2_17_0, X0_17); SCHED();
    MFMA(c2_0_1, a2_17_0, X1_17); SCHED();
    const u32x4 a3_1_1 = LDSR(pW_1, 15360);
    MFMA(c2_1_0, a2_17_1, X0_17); SCHED();
    float t8a; V_FMA(t8a, M0, v1_r1_0_0[0], v2_r1_0_0[0]);
    float t8b; V_FMA(t8b, M0, v1_r1_0_0[1], v2_r1_0_0[1]);
    const u32x4 hv_r0_1 = {hp_r0_1_0, hp_r0_1_1, hp_r0_1_2, hp_r0_1_3}; HST(sHS1 + 1024, hv_r0_1);
    MFMA(c2_1_1, a2_17_1, X1_17); SCHED();
    WAITV(16); BAR();
    const u32x4 a3_2_0 = LDSR(pW_1, 18432);
    V_FMAC(t8a, c1_0_0[0], R0);
    V_FMAC(t8b, c1_0_0[1], R0);
    f32x4 c3_0_0; MFMA0(c3_0_0, a3_0_0, X0_0); SCHED();
    float e8a; V_EXP(e8a, t8a);
    float e8b; V_EXP(e8b, t8b);
    f32x4 c3_0_1; MFMA0(c3_0_1, a3_0_0, X1_0); SCHED();
    const u32x4 a3_2_1 = LDSR(pW_1, 19456);
    V_ADD1(e8a);
    V_ADD1(e8b);
    DMA2(wsrc, 335872, 40960);
    f32x4 c3_1_0; MFMA0(c3_1_0, a3_0_1, X0_0); SCHED();
    V_RCP(e8a);
    V_RCP(e8b);
    f32x4 c3_1_1; MFMA0(c3_1_1, a3_0_1, X1_0); SCHED();
    const u32x4 a3_3_0 = LDSR(pW_1, 22528);
    V_MUL(t8a, e8a);
    MFMA(c3_0_0, a3_1_0, X0_1); SCHED();
    V_MUL(t8b, e8b);
    u32 hp_r1_0_0; V_CVT(hp_r1_0_0, t8a, t8b);
    MFMA(c3_0_1, a3_1_0, X1_1); SCHED();
    const u32x4 a3_3_1 = LDSR(pW_1, 23552);
    float t9a; V_FMA(t9a, M0, v1_r1_0_0[2], v2_r1_0_0[2]);
    float t9b; V_FMA(t9b, M0, v1_r1_0_0[3], v2_r1_0_0[3]);
    MFMA(c3_1_0, a3_1_1, X0_1); SCHED();
    const f32x4 v1_r1_0_1 = LDSRF(pV, 448), v2_r1_0_1 = LDSRF(pV, 1600);
    V_FMAC(t9a, c1_0_0[2], R0);
    V_FMAC(t9b, c1_0_0[3], R0);
    MFMA(c3_1_1, a3_1_1, X1_1); SCHED();
    const u32x4 a3_4_0 = LDSR(pW_1, 26624);
    float e9a; V_EXP(e9a, t9a);
    float e9b; V_EXP(e9b, t9b);
    MFMA(c3_0_0, a3_2_0, X0_2); SCHED();
    V_ADD1(e9a);
    V_ADD1(e9b);
    MFMA(c3_0_1, a3_2_0, X1_2); SCHED();
    const u32x4 a3_4_1 = LDSR(pW_1, 27648);
    V_RCP(e9a);
    MFMA(c3_1_0, a3_2_1, X0_2); SCHED();
    V_RCP(e9b);
    V_MUL(t9a, e9a);
    MFMA(c3_1_1, a3_2_1, X1_2); SCHED();
    const u32x4 a3_5_0 = LDSR(pW_1, 30720);
    V_MUL(t9b, e9b);
    u32 hp_r1_0_1; V_CVT(hp_r1_0_1, t9a, t9b);
    MFMA(c3_0_0, a3_3_0, X0_3); SCHED();
    float t10a; V_FMA(t10a, M0, v1_r1_0_1[0], v2_r1_0_1[0]);
    float t10b; V_FMA(t10b, M0, v1_r1_0_1[1], v2_r1_0_1[1]);
    DMA2(wsrc, 352256, 57344);
    MFMA(c3_0_1, a3_3_0, X1_3); SCHED();
    const u32x4 a3_5_1 = LDSR(pW_1, 31744);
    V_FMAC(t10a, c1_1_0[0], R0);
    V_FMAC(t10b, c1_1_0[1], R0);
    MFMA(c3_1_0, a3_3_1, X0_3); SCHED();
    float e10a; V_EXP(e10a, t10a);
    float e10b; V_EXP(e10b, t10b);
    MFMA(c3_1_1, a3_3_1, X1_3); SCHED();
    const u32x4 a3_6_0 = LDSR(pW_1, 34816);
    V_ADD1(e10a);
    MFMA(c3_0_0, a3_4_0, X0_4); SCHED();
    V_ADD1(e10b);
    V_RCP(e10a);
    MFMA(c3_0_1, a3_4_0, X1_4); SCHED();
    const u32x4 a3_6_1 = LDSR(pW_1, 35840);
    V_RCP(e10b);
    V_MUL(t10a, e10a);
    MFMA(c3_1_0, a3_4_1, X0_4); SCHED();
    V_MUL(t10b, e10b);
    u32 hp_r1_0_2; V_CVT(hp_r1_0_2, t10a, t10b);
    MFMA(c3_1_1, a3_4_1, X1_4); SCHED();
    const u32x4 a3_7_0 = LDSR(pW_1, 38912);
    float t11a; V_FMA(t11a, M0, v1_r1_0_1[2], v2_r1_0_1[2]);
    float t11b; V_FMA(t11b, M0, v1_r1_0_1[3], v2_r1_0_1[3]);
    MFMA(c3_0_0, a3_5_0, X0_5); SCHED();
    const f32x4 v1_r1_1_0 = LDSRF(pV, 384), v2_r1_1_0 = LDSRF(pV, 1536);
    V_FMAC(t11a, c1_1_0[2], R0);
    MFMA(c3_0_1, a3_5_0, X1_5); SCHED();
    const u32x4 a3_7_1 = LDSR(pW_1, 39936);
    V_FMAC(t11b, c1_1_0[3], R0);
    float e11a; V_EXP(e11a, t11a);
    MFMA(c3_1_0, a3_5_1, X0_5); SCHED();
    float e11b; V_EXP(e11b, t11b);
    V_ADD1(e11a);
    MFMA(c3_1_1, a3_5_1, X1_5); SCHED();
    WAITV(17); BAR();
    const u32x4 a3_8_0 = LDSR(pW_1, 43008);
    V_ADD1(e11b);
    V_RCP(e11a);
    MFMA(c3_0_0, a3_6_0, X0_6); SCHED();
    V_RCP(e11b);
    V_MUL(t11a, e11a);
    MFMA(c3_0_1, a3_6_0, X1_6); SCHED();
    const u32x4 a3_8_1 = LDSR(pW_1, 44032);
    V_MUL(t11b, e11b);
    u32 hp_r1_0_3; V_CVT(hp_r1_0_3, t11a, t11b);
    DMA2(wsrc, 368640, 73728);
    MFMA(c3_1_0, a3_6_1, X0_6); SCHED();
    float t12a; V_FMA(t12a, M1, v1_r1_1_0[0], v2_r1_1_0[0]);
    MFMA(c3_1_1, a3_6_1, X1_6); SCHED();
    const u32x4 a3_9_0 = LDSR(pW_1, 47104);
    float t12b; V_FMA(t12b, M1, v1_r1_1_0[1], v2_r1_1_0[1]);
    V_FMAC(t12a, c1_0_1[0], R1);
    DMA2(wsrc, 385024, 90112);
    MFMA(c3_0_0, a3_7_0, X0_7); SCHED();
    V_FMAC(t12b, c1_0_1[1], R1);
    float e12a; V_EXP(e12a, t12a);
    const u32x4 hv_r1_0 = {hp_r1_0_0, hp_r1_0_1, hp_r1_0_2, hp_r1_0_3}; HST(sHS0 + 3072, hv_r1_0);
    MFMA(c3_0_1, a3_7_0, X1_7); SCHED();
    WAITV(9); BAR();
    const u32x4 a3_9_1 = LDSR(pW_1, 48128);
    float e12b; V_EXP(e12b, t12b);
    V_ADD1(e12a);
    MFMA(c3_1_0, a3_7_1, X0_7); SCHED();
    V_ADD1(e12b);
    V_RCP(e12a);
    MFMA(c3_1_1, a3_7_1, X1_7); SCHED();
    const u32x4 a3_10_0 = LDSR(pW_1, 51200);
    V_RCP(e12b);
    V_MUL(t12a, e12a);
    MFMA(c3_0_0, a3_8_0, X0_8); SCHED();
    V_MUL(t12b, e12b);
    MFMA(c3_0_1, a3_8_0, X1_8); SCHED();
    const u32x4 a3_10_1 = LDSR(pW_1, 52224);
    u32 hp_r1_1_0; V_CVT(hp_r1_1_0, t12a, t12b);
    float t13a; V_FMA(t13a, M1, v1_r1_1_0[2], v2_r1_1_0[2]);
    MFMA(c3_1_0, a3_8_1, X0_8); SCHED();
    const f32x4 v1_r1_1_1 = LDSRF(pV, 448), v2_r1_1_1 = LDSRF(pV, 1600);
    float t13b; V_FMA(t13b, M1, v1_r1_1_0[3], v2_r1_1_0[3]);
    V_FMAC(t13a, c1_0_1[2], R1);
    MFMA(c3_1_1, a3_8_1, X1_8); SCHED();
    const u32x4 a3_11_0 = LDSR(pW_1, 55296);
    V_FMAC(t13b, c1_0_1[3], R1);
    float e13a; V_EXP(e13a, t13a);
    MFMA(c3_0_0, a3_9_0, X0_9); SCHED();
    float e13b; V_EXP(e13b, t13b);
    V_ADD1(e13a);
    MFMA(c3_0_1, a3_9_0, X1_9); SCHED();
    const u32x4 a3_11_1 = LDSR(pW_1, 56320);
    V_ADD1(e13b);
    MFMA(c3_1_0, a3_9_1, X0_9); SCHED();
    V_RCP(e13a);
    V_RCP(e13b);
    MFMA(c3_1_1, a3_9_1, X1_9); SCHED();
    const u32x4 a3_12_0 = LDSR(pW_1, 59392);
    V_MUL(t13a, e13a);
    V_MUL(t13b, e13b);
    MFMA(c3_0_0, a3_10_0, X0_10); SCHED();
    u32 hp_r1_1_1; V_CVT(hp_r1_1_1, t13a, t13b);
    float t14a; V_FMA(t14a, M1, v1_r1_1_1[0], v2_r1_1_1[0]);
    MFMA(c3_0_1, a3_10_0, X1_10); SCHED();
    const u32x4 a3_12_1 = LDSR(pW_1, 60416);
    float t14b; V_FMA(t14b, M1, v1_r1_1_1[1], v2_r1_1_1[1]);
    V_FMAC(t14a, c1_1_1[0], R1);
    MFMA(c3_1_0, a3_10_1, X0_10); SCHED();
    V_FMAC(t14b, c1_1_1[1], R1);
    float e14a; V_EXP(e14a, t14a);
    MFMA(c3_1_1, a3_10_1, X1_10); SCHED();
    const u32x4 a3_13_0 = LDSR(pW_1, 63488);
    float e14b; V_EXP(e14b, t14b);
    MFMA(c3_0_0, a3_11_0, X0_11); SCHED();
    V_ADD1(e14a);
    V_ADD1(e14b);
    MFMA(c3_0_1, a3_11_0, X1_11); SCHED();
    const u32x4 a3_13_1 = LDSR(pW_1, 64512);
    V_RCP(e14a);
    V_RCP(e14b);
    MFMA(c3_1_0, a3_11_1, X0_11); SCHED();
    V_MUL(t14a, e14a);
    V_MUL(t14b, e14b);
    MFMA(c3_1_1, a3_11_1, X1_11); SCHED();
    WAITV(16); BAR();
    const u32x4 a3_14_0 = LDSR(pW_2, 2048);
    u32 hp_r1_1_2; V_CVT(hp_r1_1_2, t14a, t14b);
    float t15a; V_FMA(t15a, M1, v1_r1_1_1[2], v2_r1_1_1[2]);
    MFMA(c3_0_0, a3_12_0, X0_12); SCHED();
    float t15b; V_FMA(t15b, M1, v1_r1_1_1[3], v2_r1_1_1[3]);
    V_FMAC(t15a, c1_1_1[2], R1);
    MFMA(c3_0_1, a3_12_0, X1_12); SCHED();
    const u32x4 a3_14_1 = LDSR(pW_2, 3072);
    V_FMAC(t15b, c1_1_1[3], R1);
    MFMA(c3_1_0, a3_12_1, X0_12); SCHED();
    float e15a; V_EXP(e15a, t15a);
    float e15b; V_EXP(e15b, t15b);
    MFMA(c3_1_1, a3_12_1, X1_12); SCHED();
    const u32x4 a3_15_0 = LDSR(pW_2, 6144);
    V_ADD1(e15a);
    V_ADD1(e15b);
    MFMA(c3_0_0, a3_13_0, X0_13); SCHED();
    V_RCP(e15a);
    V_RCP(e15b);
    MFMA(c3_0_1, a3_13_0, X1_13); SCHED();
    const f32x4 v1_r2_0_0 = LDSRF(pV, 640), v2_r2_0_0 = LDSRF(pV, 1792);
    const u32x4 a3_15_1 = LDSR(pW_2, 7168);
    V_MUL(t15a, e15a);
    V_MUL(t15b, e15b);
    MFMA(c3_1_0, a3_13_1, X0_13); SCHED();
    u32 hp_r1_1_3; V_CVT(hp_r1_1_3, t15a, t15b);
    MFMA(c3_1_1, a3_13_1, X1_13); SCHED();
    const u32x4 a3_16_0 = LDSR(pW_2, 10240);
    u32 fl1; POLL(fl1, sFL, 2);
    MFMA(c3_0_0, a3_14_0, X0_14); SCHED();
    DMA2(wsrc, 401408, 106496);
    MFMA(c3_0_1, a3_14_0, X1_14); SCHED();
    const u32x4 a3_16_1 = LDSR(pW_2, 11264);
    float t16a; V_FMA(t16a, M0, v1_r2_0_0[0], v2_r2_0_0[0]);
    float t16b; V_FMA(t16b, M0, v1_r2_0_0[1], v2_r2_0_0[1]);
    const u32x4 hv_r1_1 = {hp_r1_1_0, hp_r1_1_1, hp_r1_1_2, hp_r1_1_3}; HST(sHS1 + 3072, hv_r1_1);
    MFMA(c3_1_0, a3_14_1, X0_14); SCHED();
    V_FMAC(t16a, c2_0_0[0], R0);
    V_FMAC(t16b, c2_0_0[1], R0);
    MFMA(c3_1_1, a3_14_1, X1_14); SCHED();
    const u32x4 a3_17_0 = LDSR(pW_2, 14336);
    float e16a; V_EXP(e16a, t16a);
    float e16b; V_EXP(e16b, t16b);
    MFMA(c3_0_0, a3_15_0, X0_15); SCHED();
    V_ADD1(e16a);
    V_ADD1(e16b);
    MFMA(c3_0_1, a3_15_0, X1_15); SCHED();
    const u32x4 a3_17_1 = LDSR(pW_2, 15360);
    V_RCP(e16a);
    V_RCP(e16b);
    MFMA(c3_1_0, a3_15_1, X0_15); SCHED();
    V_MUL(t16a, e16a);
    MFMA(c3_1_1, a3_15_1, X1_15); SCHED();
    const u32x4 a8_0_0 = LDSR(pW_0, 0);
    V_MUL(t16b, e16b);
    u32 hp_r2_0_0; V_CVT(hp_r2_0_0, t16a, t16b);
    MFMA(c3_0_0, a3_16_0, X0_16); SCHED();
    const u32x4 a8_0_1 = LDSR(pW_0, 1024);
    float t17a; V_FMA(t17a, M0, v1_r2_0_0[2], v2_r2_0_0[2]);
    float t17b; V_FMA(t17b, M0, v1_r2_0_0[3], v2_r2_0_0[3]);
    MFMA(c3_0_1, a3_16_0, X1_16); SCHED();
    const f32x4 v1_r2_0_1 = LDSRF(pV, 704), v2_r2_0_1 = LDSRF(pV, 1856);
    const u32x4 a8_1_0 = LDSR(pW_0, 2048);
    V_FMAC(t17a, c2_0_0[2], R0);
    V_FMAC(t17b, c2_0_0[3], R0);
    MFMA(c3_1_0, a3_16_1, X0_16); SCHED();
    const u32x4 a8_1_1 = LDSR(pW_0, 3072);
    float e17a; V_EXP(e17a, t17a);
    float e17b; V_EXP(e17b, t17b);
    MFMA(c3_1_1, a3_16_1, X1_16); SCHED();
    const u32x4 a8_2_0 = LDSR(pW_0, 4096);
    V_ADD1(e17a);
    V_ADD1(e17b);
    MFMA(c3_0_0, a3_17_0, X0_17); SCHED();
    const u32x4 a8_2_1 = LDSR(pW_0, 5120);
    V_RCP(e17a);
    MFMA(c3_0_1, a3_17_0, X1_17); SCHED();
    const u32x4 a8_3_0 = LDSR(pW_0, 6144);
    V_RCP(e17b);
    V_MUL(t17a, e17a);
    MFMA(c3_1_0, a3_17_1, X0_17); SCHED();
    const u32x4 a8_3_1 = LDSR(pW_0, 7168);
    V_MUL(t17b, e17b);
    u32 hp_r2_0_1; V_CVT(hp_r2_0_1, t17a, t17b);
    MFMA(c3_1_1, a3_17_1, X1_17); SCHED();
    WAITV(14); BAR();
    const u32x4 a8_4_0 = LDSR(pW_0, 8192);
    float t18a; V_FMA(t18a, M0, v1_r2_0_1[0], v2_r2_0_1[0]);
    float t18b; V_FMA(t18b, M0, v1_r2_0_1[1], v2_r2_0_1[1]);
    f32x4 c8_0; MFMA0(c8_0, a8_0_0, X0_0); SCHED();
    const u32x4 a8_4_1 = LDSR(pW_0, 9216);
    V_FMAC(t18a, c2_1_0[0], R0);
    V_FMAC(t18b, c2_1_0[1], R0);
    f32x4 c8_1; MFMA0(c8_1, a8_0_1, X0_0); SCHED();
    const u32x4 a8_5_0 = LDSR(pW_0, 10240);
    float e18a; V_EXP(e18a, t18a);
    float e18b; V_EXP(e18b, t18b);
    DMA2(wsrc, 417792, 122880);
    MFMA(c8_0, a8_1_0, X0_1); SCHED();
    const u32x4 a8_5_1 = LDSR(pW_0, 11264);
    V_ADD1(e18a);
    MFMA(c8_1, a8_1_1, X0_1); SCHED();
    const u32x4 a8_6_0 = LDSR(pW_0, 12288);
    V_ADD1(e18b);
    V_RCP(e18a);
    MFMA(c8_0, a8_2_0, X0_2); SCHED();
    const u32x4 a8_6_1 = LDSR(pW_0, 13312);
    V_RCP(e18b);
    V_MUL(t18a, e18a);
    asm volatile("s_waitcnt vmcnt(5)" : "+v"(fl1)::"memory"); CHECK(fl1, 1u, sFL, 2);
    MFMA(c8_1, a8_2_1, X0_2); SCHED();
    const u32x4 a8_7_0 = LDSR(pW_0, 14336);
    V_MUL(t18b, e18b);
    u32 hp_r2_0_2; V_CVT(hp_r2_0_2, t18a, t18b);
    u32x4 h0_0; HLOAD(h0_0, sHJ0 + sHR0 + 1024);
    MFMA(c8_0, a8_3_0, X0_3); SCHED();
    const u32x4 a8_7_1 = LDSR(pW_0, 15360);
    float t19a; V_FMA(t19a, M0, v1_r2_0_1[2], v2_r2_0_1[2]);
    float t19b; V_FMA(t19b, M0, v1_r2_0_1[3], v2_r2_0_1[3]);
    u32x4 h0_1; HLOAD(h0_1, sHJ0 + sHR1 + 1024);
    MFMA(c8_1, a8_3_1, X0_3); SCHED();
    const f32x4 v1_r2_1_0 = LDSRF(pV, 640), v2_r2_1_0 = LDSRF(pV, 1792);
    const u32x4 a8_8_0 = LDSR(pW_0, 16384);
    V_FMAC(t19a, c2_1_0[2], R0);
    MFMA(c8_0, a8_4_0, X0_4); SCHED();
    const u32x4 a8_8_1 = LDSR(pW_0, 17408);
    V_FMAC(t19b, c2_1_0[3], R0);
    float e19a; V_EXP(e19a, t19a);
    MFMA(c8_1, a8_4_1, X0_4); SCHED();
    const u32x4 a8_9_0 = LDSR(pW_0, 18432);
    float e19b; V_EXP(e19b, t19b);
    V_ADD1(e19a);
    MFMA(c8_0, a8_5_0, X0_5); SCHED();
    const u32x4 a8_9_1 = LDSR(pW_0, 19456);
    V_ADD1(e19b);
    V_RCP(e19a);
    MFMA(c8_1, a8_5_1, X0_5); SCHED();
    const u32x4 a8_10_0 = LDSR(pW_0, 20480);
    V_RCP(e19b);
    V_MUL(t19a, e19a);
    MFMA(c8_0, a8_6_0, X0_6); SCHED();
    const u32x4 a8_10_1 = LDSR(pW_0, 21504);
    V_MUL(t19b, e19b);
    u32 hp_r2_0_3; V_CVT(hp_r2_0_3, t19a, t19b);
    MFMA(c8_1, a8_6_1, X0_6); SCHED();
    const u32x4 a8_11_0 = LDSR(pW_0, 22528);
    float t20a; V_FMA(t20a, M1, v1_r2_1_0[0], v2_r2_1_0[0]);
    MFMA(c8_0, a8_7_0, X0_7); SCHED();
    const u32x4 a8_11_1 = LDSR(pW_0, 23552);
    float t20b; V_FMA(t20b, M1, v1_r2_1_0[1], v2_r2_1_0[1]);
    V_FMAC(t20a, c2_0_1[0], R1);
    MFMA(c8_1, a8_7_1, X0_7); SCHED();
    const u32x4 a8_12_0 = LDSR(pW_0, 24576);
    V_FMAC(t20b, c2_0_1[1], R1);
    float e20a; V_EXP(e20a, t20a);
    const u32x4 h8_0 = {hp_r2_0_0, hp_r2_0_1, hp_r2_0_2, hp_r2_0_3}; HST(sHS0 + 5120, h8_0);
    MFMA(c8_0, a8_8_0, X0_8); SCHED();
    const u32x4 a8_12_1 = LDSR(pW_0, 25600);
    float e20b; V_EXP(e20b, t20b);
    V_ADD1(e20a);
    u32x4 h1_0; HLOAD(h1_0, sHJ1 + sHR0 + 1024);
    MFMA(c8_1, a8_8_1, X0_8); SCHED();
    WAITV(6); BAR();
    const u32x4 a8_13_0 = LDSR(pW_0, 26624);
    V_ADD1(e20b);
    V_RCP(e20a);
    u32x4 h1_1; HLOAD(h1_1, sHJ1 + sHR1 + 1024);
    MFMA(c8_0, a8_9_0, X0_9); SCHED();
    const u32x4 a8_13_1 = LDSR(pW_0, 27648);
    V_RCP(e20b);
    V_MUL(t20a, e20a);
    MFMA(c8_1, a8_9_1, X0_9); SCHED();
    const u32x4 a8_14_0 = LDSR(pW_0, 28672);
    V_MUL(t20b, e20b);
    MFMA(c8_0, a8_10_0, X0_10); SCHED();
    const u32x4 a8_14_1 = LDSR(pW_0, 29696);
    u32 hp_r2_1_0; V_CVT(hp_r2_1_0, t20a, t20b);
    float t21a; V_FMA(t21a, M1, v1_r2_1_0[2], v2_r2_1_0[2]);
    MFMA(c8_1, a8_10_1, X0_10); SCHED();
    const f32x4 v1_r2_1_1 = LDSRF(pV, 704), v2_r2_1_1 = LDSRF(pV, 1856);
    const u32x4 a8_15_0 = LDSR(pW_0, 30720);
    float t21b; V_FMA(t21b, M1, v1_r2_1_0[3], v2_r2_1_0[3]);
    V_FMAC(t21a, c2_0_1[2], R1);
    MFMA(c8_0, a8_11_0, X0_11); SCHED();
    const u32x4 a8_15_1 = LDSR(pW_0, 31744);
    V_FMAC(t21b, c2_0_1[3], R1);
    float e21a; V_EXP(e21a, t21a);
    MFMA(c8_1, a8_11_1, X0_11); SCHED();
    const u32x4 a8_16_0 = LDSR(pW_0, 32768);
    float e21b; V_EXP(e21b, t21b);
    V_ADD1(e21a);
    MFMA(c8_0, a8_12_0, X0_12); SCHED();
    const u32x4 a8_16_1 = LDSR(pW_0, 33792);
    V_ADD1(e21b);
    MFMA(c8_1, a8_12_1, X0_12); SCHED();
    const u32x4 a8_17_0 = LDSR(pW_0, 34816);
    V_RCP(e21a);
    V_RCP(e21b);
    MFMA(c8_0, a8_13_0, X0_13); SCHED();
    const u32x4 a8_17_1 = LDSR(pW_0, 35840);
    V_MUL(t21a, e21a);
    V_MUL(t21b, e21b);
    MFMA(c8_1, a8_13_1, X0_13); SCHED();
    const u32x4 w0_0 = LDSR(pW_0, 37888);
    u32 hp_r2_1_1; V_CVT(hp_r2_1_1, t21a, t21b);
    float t22a; V_FMA(t22a, M1, v1_r2_1_1[0], v2_r2_1_1[0]);
    DMA2(wsrc, 434176, 139264);
    MFMA(c8_0, a8_14_0, X0_14); SCHED();
    float t22b; V_FMA(t22b, M1, v1_r2_1_1[1], v2_r2_1_1[1]);
    V_FMAC(t22a, c2_1_1[0], R1);
    MFMA(c8_1, a8_14_1, X0_14); SCHED();
    const u32x4 w0_1 = LDSR(pW_0, 39936);
    V_FMAC(t22b, c2_1_1[1], R1);
    float e22a; V_EXP(e22a, t22a);
    MFMA(c8_0, a8_15_0, X0_15); SCHED();
    float e22b; V_EXP(e22b, t22b);
    MFMA(c8_1, a8_15_1, X0_15); SCHED();
    const u32x4 w0_2 = LDSR(pW_0, 41984);
    V_ADD1(e22a);
    V_ADD1(e22b);
    MFMA(c8_0, a8_16_0, X0_16); SCHED();
    V_RCP(e22a);
    V_RCP(e22b);
    MFMA(c8_1, a8_16_1, X0_16); SCHED();
    const u32x4 w0_3 = LDSR(pW_0, 44032);
    V_MUL(t22a, e22a);
    V_MUL(t22b, e22b);
    MFMA(c8_0, a8_17_0, X0_17); SCHED();
    u32 hp_r2_1_2; V_CVT(hp_r2_1_2, t22a, t22b);
    float t23a; V_FMA(t23a, M1, v1_r2_1_1[2], v2_r2_1_1[2]);
    u32x4 h2_0; HLOAD(h2_0, sHJ2 + sHR0 + 1024);
    MFMA(c8_1, a8_17_1, X0_17); SCHED();
    WAITV(17); BAR();
    const u32x4 w0_4 = LDSR(pW_0, 46080);
    float t23b; V_FMA(t23b, M1, v1_r2_1_1[3], v2_r2_1_1[3]);
    V_FMAC(t23a, c2_1_1[2], R1);
    u32x4 h2_1; HLOAD(h2_1, sHJ2 + sHR1 + 1024);
    asm volatile("s_waitcnt vmcnt(7)" : "+v"(h0_0), "+v"(h0_1)::"memory");
    f32x4 o0_0; MFMA0(o0_0, w0_0, h0_0); SCHED();
    V_FMAC(t23b, c2_1_1[3], R1);
    f32x4 o0_1; MFMA0(o0_1, w0_0, h0_1); SCHED();
    const u32x4 w0_5 = LDSR(pW_0, 48128);
    float e23a; V_EXP(e23a, t23a);
    float e23b; V_EXP(e23b, t23b);
    f32x4 o1_0; MFMA0(o1_0, w0_1, h0_0); SCHED();
    V_ADD1(e23a);
    V_ADD1(e23b);
    f32x4 o1_1; MFMA0(o1_1, w0_1, h0_1); SCHED();
    const u32x4 w0_6 = LDSR(pW_0, 50176);
    V_RCP(e23a);
    V_RCP(e23b);
    f32x4 o2_0; MFMA0(o2_0, w0_2, h0_0); SCHED();
    const f32x4 v1_r3_0_0 = LDSRF(pV, 896), v2_r3_0_0 = LDSRF(pV, 2048);
    V_MUL(t23a, e23a);
    V_MUL(t23b, e23b);
    f32x4 o2_1; MFMA0(o2_1, w0_2, h0_1); SCHED();
    const u32x4 w0_7 = LDSR(pW_0, 52224);
    u32 hp_r2_1_3; V_CVT(hp_r2_1_3, t23a, t23b);
    f32x4 o3_0; MFMA0(o3_0, w0_3, h0_0); SCHED();
    f32x4 o3_1; MFMA0(o3_1, w0_3, h0_1); SCHED();
    const u32x4 w0_8 = LDSR(pW_0, 54272);
    f32x4 o4_0; MFMA0(o4_0, w0_4, h0_0); SCHED();
    float t24a; V_FMA(t24a, M0, v1_r3_0_0[0], v2_r3_0_0[0]);
    float t24b; V_FMA(t24b, M0, v1_r3_0_0[1], v2_r3_0_0[1]);
    const u32x4 h8_1 = {hp_r2_1_0, hp_r2_1_1, hp_r2_1_2, hp_r2_1_3}; HST(sHS1 + 5120, h8_1);
    f32x4 o4_1; MFMA0(o4_1, w0_4, h0_1); SCHED();
    const u32x4 w1_0 = LDSR(pW_0, 56320);
    V_FMAC(t24a, c3_0_0[0], R0);
    V_FMAC(t24b, c3_0_0[1], R0);
    DMA2(wsrc, 450560, 8192);
    f32x4 o5_0; MFMA0(o5_0, w0_5, h0_0); SCHED();
    float e24a; V_EXP(e24a, t24a);
    float e24b; V_EXP(e24b, t24b);
    f32x4 o5_1; MFMA0(o5_1, w0_5, h0_1); SCHED();
    const u32x4 w1_1 = LDSR(pW_0, 58368);
    V_ADD1(e24a);
    V_ADD1(e24b);
    f32x4 o6_0; MFMA0(o6_0, w0_6, h0_0); SCHED();
    V_RCP(e24a);
    V_RCP(e24b);
    u32 fl2; POLL(fl2, sFL, 2);
    f32x4 o6_1; MFMA0(o6_1, w0_6, h0_1); SCHED();
    const u32x4 w1_2 = LDSR(pW_0, 60416);
    V_MUL(t24a, e24a);
    f32x4 o7_0; MFMA0(o7_0, w0_7, h0_0); SCHED();
    V_MUL(t24b, e24b);
    u32 hp_r3_0_0; V_CVT(hp_r3_0_0, t24a, t24b);
    f32x4 o7_1; MFMA0(o7_1, w0_7, h0_1); SCHED();
    const u32x4 w1_3 = LDSR(pW_0, 62464);
    float t25a; V_FMA(t25a, M0, v1_r3_0_0[2], v2_r3_0_0[2]);
    float t25b; V_FMA(t25b, M0, v1_r3_0_0[3], v2_r3_0_0[3]);
    f32x4 o8_0; MFMA0(o8_0, w0_8, h0_0); SCHED();
    const f32x4 v1_r3_0_1 = LDSRF(pV, 960), v2_r3_0_1 = LDSRF(pV, 2112);
    V_FMAC(t25a, c3_0_0[2], R0);
    V_FMAC(t25b, c3_0_0[3], R0);
    u32x4 h3_0; HLOAD(h3_0, sHJ3 + sHR0 + 1024);
    f32x4 o8_1; MFMA0(o8_1, w0_8, h0_1); SCHED();
    const u32x4 w1_4 = LDSR(pW_0, 64512);
    float e25a; V_EXP(e25a, t25a);
    float e25b; V_EXP(e25b, t25b);
    u32x4 h3_1; HLOAD(h3_1, sHJ3 + sHR1 + 1024);
    asm volatile("s_waitcnt vmcnt(10)" : "+v"(h1_0), "+v"(h1_1)::"memory");
    MFMA(o0_0, w1_0, h1_0); SCHED();
    V_ADD1(e25a);
    V_ADD1(e25b);
    MFMA(o0_1, w1_0, h1_1); SCHED();
    const u32x4 w1_5 = LDSR(pW_1, 1024);
    V_RCP(e25a);
    MFMA(o1_0, w1_1, h1_0); SCHED();
    V_RCP(e25b);
    V_MUL(t25a, e25a);
    MFMA(o1_1, w1_1, h1_1); SCHED();
    const u32x4 w1_6 = LDSR(pW_1, 3072);
    V_MUL(t25b, e25b);
    u32 hp_r3_0_1; V_CVT(hp_r3_0_1, t25a, t25b);
    MFMA(o2_0, w1_2, h1_0); SCHED();
    float t26a; V_FMA(t26a, M0, v1_r3_0_1[0], v2_r3_0_1[0]);
    float t26b; V_FMA(t26b, M0, v1_r3_0_1[1], v2_r3_0_1[1]);
    MFMA(o2_1, w1_2, h1_1); SCHED();
    const u32x4 w1_7 = LDSR(pW_1, 5120);
    V_FMAC(t26a, c3_1_0[0], R0);
    V_FMAC(t26b, c3_1_0[1], R0);
    MFMA(o3_0, w1_3, h1_0); SCHED();
    float e26a; V_EXP(e26a, t26a);
    float e26b; V_EXP(e26b, t26b);
    MFMA(o3_1, w1_3, h1_1); SCHED();
    const u32x4 w1_8 = LDSR(pW_1, 7168);
    V_ADD1(e26a);
    MFMA(o4_0, w1_4, h1_0); SCHED();
    V_ADD1(e26b);
    V_RCP(e26a);
    DMA2(wsrc, 466944, 24576);
    MFMA(o4_1, w1_4, h1_1); SCHED();
    const u32x4 w2_0 = LDSR(pW_1, 9216);
    V_RCP(e26b);
    V_MUL(t26a, e26a);
    MFMA(o5_0, w1_5, h1_0); SCHED();
    V_MUL(t26b, e26b);
    u32 hp_r3_0_2; V_CVT(hp_r3_0_2, t26a, t26b);
    MFMA(o5_1, w1_5, h1_1); SCHED();
    const u32x4 w2_1 = LDSR(pW_1, 11264);
    float t27a; V_FMA(t27a, M0, v1_r3_0_1[2], v2_r3_0_1[2]);
    float t27b; V_FMA(t27b, M0, v1_r3_0_1[3], v2_r3_0_1[3]);
    MFMA(o6_0, w1_6, h1_0); SCHED();
    const f32x4 v1_r3_1_0 = LDSRF(pV, 896), v2_r3_1_0 = LDSRF(pV, 2048);
    V_FMAC(t27a, c3_1_0[2], R0);
    MFMA(o6_1, w1_6, h1_1); SCHED();
    const u32x4 w2_2 = LDSR(pW_1, 13312);
    V_FMAC(t27b, c3_1_0[3], R0);
    float e27a; V_EXP(e27a, t27a);
    MFMA(o7_0, w1_7, h1_0); SCHED();
    float e27b; V_EXP(e27b, t27b);
    V_ADD1(e27a);
    MFMA(o7_1, w1_7, h1_1); SCHED();
    const u32x4 w2_3 = LDSR(pW_1, 15360);
    V_ADD1(e27b);
    V_RCP(e27a);
    asm volatile("s_waitcnt vmcnt(4)" : "+v"(fl2)::"memory"); CHECK(fl2, 2u, sFL, 2);
    MFMA(o8_0, w1_8, h1_0); SCHED();
    V_RCP(e27b);
    V_MUL(t27a, e27a);
    u32x4 h4_0; HLOAD(h4_0, sHJ0 + sHR0 + 3072);
    MFMA(o8_1, w1_8, h1_1); SCHED();
    WAITV(27); BAR();
    const u32x4 w2_4 = LDSR(pW_1, 17408);
    V_MUL(t27b, e27b);
    u32 hp_r3_0_3; V_CVT(hp_r3_0_3, t27a, t27b);
    u32x4 h4_1; HLOAD(h4_1, sHJ0 + sHR1 + 3072);
    asm volatile("s_waitcnt vmcnt(10)" : "+v"(h2_0), "+v"(h2_1)::"memory");
    MFMA(o0_0, w2_0, h2_0); SCHED();
    float t28a; V_FMA(t28a, M1, v1_r3_1_0[0], v2_r3_1_0[0]);
    MFMA(o0_1, w2_0, h2_1); SCHED();
    const u32x4 w2_5 = LDSR(pW_1, 19456);
    float t28b; V_FMA(t28b, M1, v1_r3_1_0[1], v2_r3_1_0[1]);
    V_FMAC(t28a, c3_0_1[0], R1);
    MFMA(o1_0, w2_1, h2_0); SCHED();
    V_FMAC(t28b, c3_0_1[1], R1);
    float e28a; V_EXP(e28a, t28a);
    const u32x4 h12_0 = {hp_r3_0_0, hp_r3_0_1, hp_r3_0_2, hp_r3_0_3}; HST(sHS0 + 7168, h12_0);
    MFMA(o1_1, w2_1, h2_1); SCHED();
    const u32x4 w2_6 = LDSR(pW_1, 21504);
    float e28b; V_EXP(e28b, t28b);
    V_ADD1(e28a);
    MFMA(o2_0, w2_2, h2_0); SCHED();
    V_ADD1(e28b);
    V_RCP(e28a);
    MFMA(o2_1, w2_2, h2_1); SCHED();
    const u32x4 w2_7 = LDSR(pW_1, 23552);
    V_RCP(e28b);
    V_MUL(t28a, e28a);
    MFMA(o3_0, w2_3, h2_0); SCHED();
    V_MUL(t28b, e28b);
    MFMA(o3_1, w2_3, h2_1); SCHED();
    WAITV(10); BAR();
    const u32x4 w2_8 = LDSR(pW_1, 25600);
    u32 hp_r3_1_0; V_CVT(hp_r3_1_0, t28a, t28b);
    float t29a; V_FMA(t29a, M1, v1_r3_1_0[2], v2_r3_1_0[2]);
    MFMA(o4_0, w2_4, h2_0); SCHED();
    const f32x4 v1_r3_1_1 = LDSRF(pV, 960), v2_r3_1_1 = LDSRF(pV, 2112);
    float t29b; V_FMA(t29b, M1, v1_r3_1_0[3], v2_r3_1_0[3]);
    V_FMAC(t29a, c3_0_1[2], R1);
    MFMA(o4_1, w2_4, h2_1); SCHED();
    const u32x4 w3_0 = LDSR(pW_1, 27648);
    V_FMAC(t29b, c3_0_1[3], R1);
    float e29a; V_EXP(e29a, t29a);
    DMA2(wsrc, 483328, 40960);
    MFMA(o5_0, w2_5, h2_0); SCHED();
    float e29b; V_EXP(e29b, t29b);
    V_ADD1(e29a);
    MFMA(o5_1, w2_5, h2_1); SCHED();
    const u32x4 w3_1 = LDSR(pW_1, 29696);
    V_ADD1(e29b);
    MFMA(o6_0, w2_6, h2_0); SCHED();
    V_RCP(e29a);
    V_RCP(e29b);
    MFMA(o6_1, w2_6, h2_1); SCHED();
    const u32x4 w3_2 = LDSR(pW_1, 31744);
    V_MUL(t29a, e29a);
    V_MUL(t29b, e29b);
    MFMA(o7_0, w2_7, h2_0); SCHED();
    u32 hp_r3_1_1; V_CVT(hp_r3_1_1, t29a, t29b);
    float t30a; V_FMA(t30a, M1, v1_r3_1_1[0], v2_r3_1_1[0]);
    MFMA(o7_1, w2_7, h2_1); SCHED();
    const u32x4 w3_3 = LDSR(pW_1, 33792);
    float t30b; V_FMA(t30b, M1, v1_r3_1_1[1], v2_r3_1_1[1]);
    V_FMAC(t30a, c3_1_1[0], R1);
    MFMA(o8_0, w2_8, h2_0); SCHED();
    V_FMAC(t30b, c3_1_1[1], R1);
    float e30a; V_EXP(e30a, t30a);
    u32x4 h5_0; HLOAD(h5_0, sHJ1 + sHR0 + 3072);
    MFMA(o8_1, w2_8, h2_1); SCHED();
    const u32x4 w3_4 = LDSR(pW_1, 35840);
    float e30b; V_EXP(e30b, t30b);
    u32x4 h5_1; HLOAD(h5_1, sHJ1 + sHR1 + 3072);
    asm volatile("s_waitcnt vmcnt(9)" : "+v"(h3_0), "+v"(h3_1)::"memory");
    MFMA(o0_0, w3_0, h3_0); SCHED();
    V_ADD1(e30a);
    V_ADD1(e30b);
    MFMA(o0_1, w3_0, h3_1); SCHED();
    const u32x4 w3_5 = LDSR(pW_1, 37888);
    V_RCP(e30a);
    V_RCP(e30b);
    MFMA(o1_0, w3_1, h3_0); SCHED();
    V_MUL(t30a, e30a);
    V_MUL(t30b, e30b);
    MFMA(o1_1, w3_1, h3_1); SCHED();
    const u32x4 w3_6 = LDSR(pW_1, 39936);
    u32 hp_r3_1_2; V_CVT(hp_r3_1_2, t30a, t30b);
    float t31a; V_FMA(t31a, M1, v1_r3_1_1[2], v2_r3_1_1[2]);
    MFMA(o2_0, w3_2, h3_0); SCHED();
    float t31b; V_FMA(t31b, M1, v1_r3_1_1[3], v2_r3_1_1[3]);
    V_FMAC(t31a, c3_1_1[2], R1);
    MFMA(o2_1, w3_2, h3_1); SCHED();
    const u32x4 w3_7 = LDSR(pW_1, 41984);
    V_FMAC(t31b, c3_1_1[3], R1);
    MFMA(o3_0, w3_3, h3_0); SCHED();
    float e31a; V_EXP(e31a, t31a);
    float e31b; V_EXP(e31b, t31b);
    MFMA(o3_1, w3_3, h3_1); SCHED();
    const u32x4 w3_8 = LDSR(pW_1, 44032);
    V_ADD1(e31a);
    V_ADD1(e31b);
    MFMA(o4_0, w3_4, h3_0); SCHED();
    V_RCP(e31a);
    V_RCP(e31b);
    DMA2(wsrc, 499712, 57344);
    MFMA(o4_1, w3_4, h3_1); SCHED();
    const f32x4 v1_b8_0_0 = LDSRF(pV, 1024), v2_b8_0_0 = LDSRF(pV, 2176);
    const u32x4 w4_0 = LDSR(pW_1, 46080);
    V_MUL(t31a, e31a);
    V_MUL(t31b, e31b);
    MFMA(o5_0, w3_5, h3_0); SCHED();
    u32 hp_r3_1_3; V_CVT(hp_r3_1_3, t31a, t31b);
    MFMA(o5_1, w3_5, h3_1); SCHED();
    const u32x4 w4_1 = LDSR(pW_1, 48128);
    MFMA(o6_0, w3_6, h3_0); SCHED();
    MFMA(o6_1, w3_6, h3_1); SCHED();
    const u32x4 w4_2 = LDSR(pW_1, 50176);
    float t32a; V_FMA(t32a, M0, v1_b8_0_0[0], v2_b8_0_0[0]);
    float t32b; V_FMA(t32b, M0, v1_b8_0_0[1], v2_b8_0_0[1]);
    V_FMAC(t32a, c8_0[0], R0);
    const u32x4 h12_1 = {hp_r3_1_0, hp_r3_1_1, hp_r3_1_2, hp_r3_1_3}; HST(sHS1 + 7168, h12_1);
    MFMA(o7_0, w3_7, h3_0); SCHED();
    V_FMAC(t32b, c8_0[1], R0);
    float e32a; V_EXP(e32a, t32a);
    MFMA(o7_1, w3_7, h3_1); SCHED();
    const u32x4 w4_3 = LDSR(pW_1, 52224);
    float e32b; V_EXP(e32b, t32b);
    V_ADD1(e32a);
    MFMA(o8_0, w3_8, h3_0); SCHED();
    V_ADD1(e32b);
    V_RCP(e32a);
    u32x4 h6_0; HLOAD(h6_0, sHJ2 + sHR0 + 3072);
    MFMA(o8_1, w3_8, h3_1); SCHED();
    WAITV(21); BAR();
    const u32x4 w4_4 = LDSR(pW_1, 54272);
    V_RCP(e32b);
    V_MUL(t32a, e32a);
    u32x4 h6_1; HLOAD(h6_1, sHJ2 + sHR1 + 3072);
    asm volatile("s_waitcnt vmcnt(10)" : "+v"(h4_0), "+v"(h4_1)::"memory");
    MFMA(o0_0, w4_0, h4_0); SCHED();
    V_MUL(t32b, e32b);
    u32 hp_b8_0_0; V_CVT(hp_b8_0_0, t32a, t32b);
    MFMA(o0_1, w4_0, h4_1); SCHED();
    const f32x4 v1_b8_0_1 = LDSRF(pV, 1088), v2_b8_0_1 = LDSRF(pV, 2240);
    const u32x4 w4_5 = LDSR(pW_1, 56320);
    float t33a; V_FMA(t33a, M0, v1_b8_0_0[2], v2_b8_0_0[2]);
    float t33b; V_FMA(t33b, M0, v1_b8_0_0[3], v2_b8_0_0[3]);
    MFMA(o1_0, w4_1, h4_0); SCHED();
    V_FMAC(t33a, c8_0[2], R0);
    V_FMAC(t33b, c8_0[3], R0);
    MFMA(o1_1, w4_1, h4_1); SCHED();
    const u32x4 w4_6 = LDSR(pW_1, 58368);
    float e33a; V_EXP(e33a, t33a);
    float e33b; V_EXP(e33b, t33b);
    MFMA(o2_0, w4_2, h4_0); SCHED();
    V_ADD1(e33a);
    V_ADD1(e33b);
    MFMA(o2_1, w4_2, h4_1); SCHED();
    const u32x4 w4_7 = LDSR(pW_1, 60416);
    V_RCP(e33a);
    V_RCP(e33b);
    MFMA(o3_0, w4_3, h4_0); SCHED();
    V_MUL(t33a, e33a);
    V_MUL(t33b, e33b);
    MFMA(o3_1, w4_3, h4_1); SCHED();
    const u32x4 w4_8 = LDSR(pW_1, 62464);
    u32 hp_b8_0_1; V_CVT(hp_b8_0_1, t33a, t33b);
    float t34a; V_FMA(t34a, M0, v1_b8_0_1[0], v2_b8_0_1[0]);
    float t34b; V_FMA(t34b, M0, v1_b8_0_1[1], v2_b8_0_1[1]);
    MFMA(o4_0, w4_4, h4_0); SCHED();
    V_FMAC(t34a, c8_1[0], R0);
    V_FMAC(t34b, c8_1[1], R0);
    MFMA(o4_1, w4_4, h4_1); SCHED();
    const u32x4 w5_0 = LDSR(pW_1, 64512);
    float e34a; V_EXP(e34a, t34a);
    float e34b; V_EXP(e34b, t34b);
    DMA2(wsrc, 516096, 73728);
    MFMA(o5_0, w4_5, h4_0); SCHED();
    V_ADD1(e34a);
    V_ADD1(e34b);
    MFMA(o5_1, w4_5, h4_1); SCHED();
    const u32x4 w5_1 = LDSR(pW_2, 1024);
    V_RCP(e34a);
    V_RCP(e34b);
    MFMA(o6_0, w4_6, h4_0); SCHED();
    V_MUL(t34a, e34a);
    V_MUL(t34b, e34b);
    MFMA(o6_1, w4_6, h4_1); SCHED();
    const u32x4 w5_2 = LDSR(pW_2, 3072);
    u32 hp_b8_0_2; V_CVT(hp_b8_0_2, t34a, t34b);
    float t35a; V_FMA(t35a, M0, v1_b8_0_1[2], v2_b8_0_1[2]);
    MFMA(o7_0, w4_7, h4_0); SCHED();
    float t35b; V_FMA(t35b, M0, v1_b8_0_1[3], v2_b8_0_1[3]);
    V_FMAC(t35a, c8_1[2], R0);
    MFMA(o7_1, w4_7, h4_1); SCHED();
    const u32x4 w5_3 = LDSR(pW_2, 5120);
    V_FMAC(t35b, c8_1[3], R0);
    float e35a; V_EXP(e35a, t35a);
    MFMA(o8_0, w4_8, h4_0); SCHED();
    float e35b; V_EXP(e35b, t35b);
    V_ADD1(e35a);
    u32x4 h7_0; HLOAD(h7_0, sHJ3 + sHR0 + 3072);
    MFMA(o8_1, w4_8, h4_1); SCHED();
    const u32x4 w5_4 = LDSR(pW_2, 7168);
    V_ADD1(e35b);
    V_RCP(e35a);
    u32x4 h7_1; HLOAD(h7_1, sHJ3 + sHR1 + 3072);
    asm volatile("s_waitcnt vmcnt(9)" : "+v"(h5_0), "+v"(h5_1)::"memory");
    MFMA(o0_0, w5_0, h5_0); SCHED();
    V_RCP(e35b);
    V_MUL(t35a, e35a);
    MFMA(o0_1, w5_0, h5_1); SCHED();
    const u32x4 w5_5 = LDSR(pW_2, 9216);
    V_MUL(t35b, e35b);
    u32 hp_b8_0_3; V_CVT(hp_b8_0_3, t35a, t35b);
    MFMA(o1_0, w5_1, h5_0); SCHED();
    MFMA(o1_1, w5_1, h5_1); SCHED();
    const u32x4 w5_6 = LDSR(pW_2, 11264);
    MFMA(o2_0, w5_2, h5_0); SCHED();
    const u32x4 hv_b8_0 = {hp_b8_0_0, hp_b8_0_1, hp_b8_0_2, hp_b8_0_3}; HST(sHS0 + 8192, hv_b8_0);
    MFMA(o2_1, w5_2, h5_1); SCHED();
    const u32x4 w5_7 = LDSR(pW_2, 13312);
    MFMA(o3_0, w5_3, h5_0); SCHED();
    MFMA(o3_1, w5_3, h5_1); SCHED();
    const u32x4 w5_8 = LDSR(pW_2, 15360);
    MFMA(o4_0, w5_4, h5_0); SCHED();
    DMA2(wsrc, 532480, 90112);
    u32 fl3; POLL(fl3, sFL, 2);
    MFMA(o4_1, w5_4, h5_1); SCHED();
    const u32x4 w6_0 = LDSR(pW_0, 1024);
    MFMA(o5_0, w5_5, h5_0); SCHED();
    MFMA(o5_1, w5_5, h5_1); SCHED();
    const u32x4 w6_1 = LDSR(pW_0, 3072);
    MFMA(o6_0, w5_6, h5_0); SCHED();
    MFMA(o6_1, w5_6, h5_1); SCHED();
    const u32x4 w6_2 = LDSR(pW_0, 5120);
    MFMA(o7_0, w5_7, h5_0); SCHED();
    MFMA(o7_1, w5_7, h5_1); SCHED();
    const u32x4 w6_3 = LDSR(pW_0, 7168);
    MFMA(o8_0, w5_8, h5_0); SCHED();
    MFMA(o8_1, w5_8, h5_1); SCHED();
    WAITV(20); BAR();
    const u32x4 w6_4 = LDSR(pW_0, 9216);
    asm volatile("s_waitcnt vmcnt(8)" : "+v"(h6_0), "+v"(h6_1)::"memory");
    MFMA(o0_0, w6_0, h6_0); SCHED();
    MFMA(o0_1, w6_0, h6_1); SCHED();
    const u32x4 w6_5 = LDSR(pW_0, 11264);
    MFMA(o1_0, w6_1, h6_0); SCHED();
    MFMA(o1_1, w6_1, h6_1); SCHED();
    const u32x4 w6_6 = LDSR(pW_0, 13312);
    MFMA(o2_0, w6_2, h6_0); SCHED();
    MFMA(o2_1, w6_2, h6_1); SCHED();
    const u32x4 w6_7 = LDSR(pW_0, 15360);
    MFMA(o3_0, w6_3, h6_0); SCHED();
    MFMA(o3_1, w6_3, h6_1); SCHED();
    const u32x4 w6_8 = LDSR(pW_0, 17408);
    MFMA(o4_0, w6_4, h6_0); SCHED();
    MFMA(o4_1, w6_4, h6_1); SCHED();
    const u32x4 w7_0 = LDSR(pW_0, 19456);
    MFMA(o5_0, w6_5, h6_0); SCHED();
    XRES1();
    MFMA(o5_1, w6_5, h6_1); SCHED();
    const u32x4 w7_1 = LDSR(pW_0, 21504);
    asm volatile("s_waitcnt vmcnt(1)" : "+v"(fl3)::"memory"); CHECK(fl3, 3u, sFL, 2);
    MFMA(o6_0, w6_6, h6_0); SCHED();
    u32x4 h9_0; HLOAD(h9_0, sHJ1 + sHR0 + 5120);
    MFMA(o6_1, w6_6, h6_1); SCHED();
    const u32x4 w7_2 = LDSR(pW_0, 23552);
    u32x4 h9_1; HLOAD(h9_1, sHJ1 + sHR1 + 5120);
    MFMA(o7_0, w6_7, h6_0); SCHED();
    MFMA(o7_1, w6_7, h6_1); SCHED();
    const u32x4 w7_3 = LDSR(pW_0, 25600);
    MFMA(o8_0, w6_8, h6_0); SCHED();
    MFMA(o8_1, w6_8, h6_1); SCHED();
    WAITV(6); BAR();
    const u32x4 w7_4 = LDSR(pW_0, 27648);
    asm volatile("s_waitcnt vmcnt(7)" : "+v"(h7_0), "+v"(h7_1)::"memory");
    MFMA(o0_0, w7_0, h7_0); SCHED();
    MFMA(o0_1, w7_0, h7_1); SCHED();
    const u32x4 w7_5 = LDSR(pW_0, 29696);
    DMA2(wsrc, 548864, 106496);
    MFMA(o1_0, w7_1, h7_0); SCHED();
    MFMA(o1_1, w7_1, h7_1); SCHED();
    const u32x4 w7_6 = LDSR(pW_0, 31744);
    MFMA(o2_0, w7_2, h7_0); SCHED();
    MFMA(o2_1, w7_2, h7_1); SCHED();
    const u32x4 w7_7 = LDSR(pW_0, 33792);
    MFMA(o3_0, w7_3, h7_0); SCHED();
    MFMA(o3_1, w7_3, h7_1); SCHED();
    const u32x4 w7_8 = LDSR(pW_0, 35840);
    MFMA(o4_0, w7_4, h7_0); SCHED();
    u32x4 h10_0; HLOAD(h10_0, sHJ2 + sHR0 + 5120);
    MFMA(o4_1, w7_4, h7_1); SCHED();
    const u32x4 w8_0 = LDSR(pW_0, 37888);
    u32x4 h10_1; HLOAD(h10_1, sHJ2 + sHR1 + 5120);
    MFMA(o5_0, w7_5, h7_0); SCHED();
    MFMA(o5_1, w7_5, h7_1); SCHED();
    const u32x4 w8_1 = LDSR(pW_0, 39936);
    MFMA(o6_0, w7_6, h7_0); SCHED();
    MFMA(o6_1, w7_6, h7_1); SCHED();
    const u32x4 w8_2 = LDSR(pW_0, 41984);
    MFMA(o7_0, w7_7, h7_0); SCHED();
    MFMA(o7_1, w7_7, h7_1); SCHED();
    const u32x4 w8_3 = LDSR(pW_0, 44032);
    MFMA(o8_0, w7_8, h7_0); SCHED();
    MFMA(o8_1, w7_8, h7_1); SCHED();
    WAITV(13); BAR();
    const u32x4 w8_4 = LDSR(pW_0, 46080);
    MFMA(o0_0, w8_0, h8_0); SCHED();
    MFMA(o0_1, w8_0, h8_1); SCHED();
    const u32x4 w8_5 = LDSR(pW_0, 48128);
    DMA2(wsrc, 565248, 122880);
    MFMA(o1_0, w8_1, h8_0); SCHED();
    MFMA(o1_1, w8_1, h8_1); SCHED();
    const u32x4 w8_6 = LDSR(pW_0, 50176);
    MFMA(o2_0, w8_2, h8_0); SCHED();
    u32x4 h11_0; HLOAD(h11_0, sHJ3 + sHR0 + 5120);
    MFMA(o2_1, w8_2, h8_1); SCHED();
    const u32x4 w9_0 = LDSR(pW_0, 52224);
    u32x4 h11_1; HLOAD(h11_1, sHJ3 + sHR1 + 5120);
    MFMA(o3_0, w8_3, h8_0); SCHED();
    MFMA(o3_1, w8_3, h8_1); SCHED();
    const u32x4 w9_1 = LDSR(pW_0, 54272);
    MFMA(o4_0, w8_4, h8_0); SCHED();
    MFMA(o4_1, w8_4, h8_1); SCHED();
    const u32x4 w9_2 = LDSR(pW_0, 56320);
    MFMA(o5_0, w8_5, h8_0); SCHED();
    u32 fl4; POLL(fl4, sFL, 2);
    MFMA(o5_1, w8_5, h8_1); SCHED();
    const u32x4 w9_3 = LDSR(pW_0, 58368);
    MFMA(o6_0, w8_6, h8_0); SCHED();
    MFMA(o6_1, w8_6, h8_1); SCHED();
    const u32x4 w9_4 = LDSR(pW_0, 60416);
    asm volatile("s_waitcnt vmcnt(9)" : "+v"(h9_0), "+v"(h9_1)::"memory");
    MFMA(o0_0, w9_0, h9_0); SCHED();
    MFMA(o0_1, w9_0, h9_1); SCHED();
    const u32x4 w9_5 = LDSR(pW_0, 62464);
    MFMA(o1_0, w9_1, h9_0); SCHED();
    MFMA(o1_1, w9_1, h9_1); SCHED();
    const u32x4 w9_6 = LDSR(pW_0, 64512);
    MFMA(o2_0, w9_2, h9_0); SCHED();
    DMA2(wsrc, 581632, 139264);
    MFMA(o2_1, w9_2, h9_1); SCHED();
    const u32x4 w10_0 = LDSR(pW_1, 1024);
    MFMA(o3_0, w9_3, h9_0); SCHED();
    MFMA(o3_1, w9_3, h9_1); SCHED();
    const u32x4 w10_1 = LDSR(pW_1, 3072);
    MFMA(o4_0, w9_4, h9_0); SCHED();
    MFMA(o4_1, w9_4, h9_1); SCHED();
    const u32x4 w10_2 = LDSR(pW_1, 5120);
    MFMA(o5_0, w9_5, h9_0); SCHED();
    MFMA(o5_1, w9_5, h9_1); SCHED();
    const u32x4 w10_3 = LDSR(pW_1, 7168);
    MFMA(o6_0, w9_6, h9_0); SCHED();
    MFMA(o6_1, w9_6, h9_1); SCHED();
    const u32x4 w10_4 = LDSR(pW_1, 9216);
    asm volatile("s_waitcnt vmcnt(7)" : "+v"(h10_0), "+v"(h10_1)::"memory");
    MFMA(o0_0, w10_0, h10_0); SCHED();
    MFMA(o0_1, w10_0, h10_1); SCHED();
    const u32x4 w10_5 = LDSR(pW_1, 11264);
    MFMA(o1_0, w10_1, h10_0); SCHED();
    MFMA(o1_1, w10_1, h10_1); SCHED();
    const u32x4 w10_6 = LDSR(pW_1, 13312);
    asm volatile("s_waitcnt vmcnt(2)" : "+v"(fl4)::"memory"); CHECK(fl4, 4u, sFL, 2);
    MFMA(o2_0, w10_2, h10_0); SCHED();
    u32x4 h13_0; HLOAD(h13_0, sHJ1 + sHR0 + 7168);
    MFMA(o2_1, w10_2, h10_1); SCHED();
    const u32x4 w11_0 = LDSR(pW_1, 15360);
    u32x4 h13_1; HLOAD(h13_1, sHJ1 + sHR1 + 7168);
    MFMA(o3_0, w10_3, h10_0); SCHED();
    MFMA(o3_1, w10_3, h10_1); SCHED();
    WAITV(11); BAR();
    const u32x4 w11_1 = LDSR(pW_1, 17408);
    MFMA(o4_0, w10_4, h10_0); SCHED();
    MFMA(o4_1, w10_4, h10_1); SCHED();
    const u32x4 w11_2 = LDSR(pW_1, 19456);
    DMA2(wsrc, 598016, 8192);
    MFMA(o5_0, w10_5, h10_0); SCHED();
    MFMA(o5_1, w10_5, h10_1); SCHED();
    const u32x4 w11_3 = LDSR(pW_1, 21504);
    MFMA(o6_0, w10_6, h10_0); SCHED();
    MFMA(o6_1, w10_6, h10_1); SCHED();
    const u32x4 w11_4 = LDSR(pW_1, 23552);
    asm volatile("s_waitcnt vmcnt(7)" : "+v"(h11_0), "+v"(h11_1)::"memory");
    MFMA(o0_0, w11_0, h11_0); SCHED();
    MFMA(o0_1, w11_0, h11_1); SCHED();
    const u32x4 w11_5 = LDSR(pW_1, 25600);
    MFMA(o1_0, w11_1, h11_0); SCHED();
    MFMA(o1_1, w11_1, h11_1); SCHED();
    const u32x4 w11_6 = LDSR(pW_1, 27648);
    MFMA(o2_0, w11_2, h11_0); SCHED();
    u32x4 h14_0; HLOAD(h14_0, sHJ2 + sHR0 + 7168);
    MFMA(o2_1, w11_2, h11_1); SCHED();
    const u32x4 w12_0 = LDSR(pW_1, 29696);
    u32x4 h14_1; HLOAD(h14_1, sHJ2 + sHR1 + 7168);
    MFMA(o3_0, w11_3, h11_0); SCHED();
    MFMA(o3_1, w11_3, h11_1); SCHED();
    const u32x4 w12_1 = LDSR(pW_1, 31744);
    MFMA(o4_0, w11_4, h11_0); SCHED();
    MFMA(o4_1, w11_4, h11_1); SCHED();
    const u32x4 w12_2 = LDSR(pW_1, 33792);
    MFMA(o5_0, w11_5, h11_0); SCHED();
    MFMA(o5_1, w11_5, h11_1); SCHED();
    const u32x4 w12_3 = LDSR(pW_1, 35840);
    MFMA(o6_0, w11_6, h11_0); SCHED();
    DMA2(wsrc, 614400, 24576);
    MFMA(o6_1, w11_6, h11_1); SCHED();
    const u32x4 w12_4 = LDSR(pW_1, 37888);
    u32x4 h15_0; HLOAD(h15_0, sHJ3 + sHR0 + 7168);
    MFMA(o0_0, w12_0, h12_0); SCHED();
    u32x4 h15_1; HLOAD(h15_1, sHJ3 + sHR1 + 7168);
    MFMA(o0_1, w12_0, h12_1); SCHED();
    const u32x4 w12_5 = LDSR(pW_1, 39936);
    MFMA(o1_0, w12_1, h12_0); SCHED();
    MFMA(o1_1, w12_1, h12_1); SCHED();
    const u32x4 w12_6 = LDSR(pW_1, 41984);
    MFMA(o2_0, w12_2, h12_0); SCHED();
    u32x4 h16_0; HLOAD(h16_0, sHR0 + 17408);
    MFMA(o2_1, w12_2, h12_1); SCHED();
    const u32x4 w13_0 = LDSR(pW_1, 44032);
    u32x4 h16_1; HLOAD(h16_1, sHR1 + 17408);
    MFMA(o3_0, w12_3, h12_0); SCHED();
    MFMA(o3_1, w12_3, h12_1); SCHED();
    const u32x4 w13_1 = LDSR(pW_1, 46080);
    MFMA(o4_0, w12_4, h12_0); SCHED();
    MFMA(o4_1, w12_4, h12_1); SCHED();
    const u32x4 w13_2 = LDSR(pW_1, 48128);
    u32x4 h17_0; HLOAD(h17_0, sHR0 + 35840);
    MFMA(o5_0, w12_5, h12_0); SCHED();
    u32x4 h17_1; HLOAD(h17_1, sHR1 + 35840);
    MFMA(o5_1, w12_5, h12_1); SCHED();
    const u32x4 w13_3 = LDSR(pW_1, 50176);
    MFMA(o6_0, w12_6, h12_0); SCHED();
    MFMA(o6_1, w12_6, h12_1); SCHED();
    const u32x4 w13_4 = LDSR(pW_1, 52224);
    asm volatile("s_waitcnt vmcnt(12)" : "+v"(h13_0), "+v"(h13_1)::"memory");
    MFMA(o0_0, w13_0, h13_0); SCHED();
    MFMA(o0_1, w13_0, h13_1); SCHED();
    WAITV(14); BAR();
    const u32x4 w13_5 = LDSR(pW_1, 54272);
    MFMA(o1_0, w13_1, h13_0); SCHED();
    u32 fl5; POLL(fl5, sFQ, 4);
    MFMA(o1_1, w13_1, h13_1); SCHED();
    const u32x4 w13_6 = LDSR(pW_1, 56320);
    DMA2(wsrc, 630784, 40960);
    MFMA(o2_0, w13_2, h13_0); SCHED();
    MFMA(o2_1, w13_2, h13_1); SCHED();
    const u32x4 w14_0 = LDSR(pW_1, 58368);
    MFMA(o3_0, w13_3, h13_0); SCHED();
    MFMA(o3_1, w13_3, h13_1); SCHED();
    const u32x4 w14_1 = LDSR(pW_1, 60416);
    MFMA(o4_0, w13_4, h13_0); SCHED();
    MFMA(o4_1, w13_4, h13_1); SCHED();
    const u32x4 w14_2 = LDSR(pW_1, 62464);
    MFMA(o5_0, w13_5, h13_0); SCHED();
    MFMA(o5_1, w13_5, h13_1); SCHED();
    const u32x4 w14_3 = LDSR(pW_1, 64512);
    MFMA(o6_0, w13_6, h13_0); SCHED();
    MFMA(o6_1, w13_6, h13_1); SCHED();
    const u32x4 w14_4 = LDSR(pW_2, 1024);
    asm volatile("s_waitcnt vmcnt(11)" : "+v"(h14_0), "+v"(h14_1)::"memory");
    MFMA(o0_0, w14_0, h14_0); SCHED();
    MFMA(o0_1, w14_0, h14_1); SCHED();
    const u32x4 w14_5 = LDSR(pW_2, 3072);
    MFMA(o1_0, w14_1, h14_0); SCHED();
    MFMA(o1_1, w14_1, h14_1); SCHED();
    const u32x4 w14_6 = LDSR(pW_2, 5120);
    MFMA(o2_0, w14_2, h14_0); SCHED();
    MFMA(o2_1, w14_2, h14_1); SCHED();
    const u32x4 w15_6 = LDSR(pW_2, 7168);
    DMA2(wsrc, 647168, 57344);
    MFMA(o3_0, w14_3, h14_0); SCHED();
    MFMA(o3_1, w14_3, h14_1); SCHED();
    const u32x4 w16_6 = LDSR(pW_2, 9216);
    MFMA(o4_0, w14_4, h14_0); SCHED();
    MFMA(o4_1, w14_4, h14_1); SCHED();
    const u32x4 w17_6 = LDSR(pW_2, 11264);
    MFMA(o5_0, w14_5, h14_0); SCHED();
    MFMA(o5_1, w14_5, h14_1); SCHED();
    const u32x4 w15_0 = LDSR(pW_2, 13312);
    MFMA(o6_0, w14_6, h14_0); SCHED();
    MFMA(o6_1, w14_6, h14_1); SCHED();
    WAITV(15); BAR();
    u32 pE_0 = l16; OPQ(pE_0); pE_0 += lds0 + rg * 1024; OPQ(pE_0);
    const u32x4 w16_0 = LDSR(pW_2, 15360);
    asm volatile("s_waitcnt vmcnt(9)" : "+v"(h15_0), "+v"(h15_1)::"memory");
    MFMA(o6_0, w15_6, h15_0); SCHED();
    MFMA(o6_1, w15_6, h15_1); SCHED();
    const u32x4 w17_0 = LDSR(pW_0, 1024);
    asm volatile("s_waitcnt vmcnt(7)" : "+v"(h16_0), "+v"(h16_1)::"memory");
    MFMA(o6_0, w16_6, h16_0); SCHED();
    MFMA(o6_1, w16_6, h16_1); SCHED();
    const u32x4 w15_1 = LDSR(pW_0, 3072);
    asm volatile("s_waitcnt vmcnt(5)" : "+v"(h17_0), "+v"(h17_1)::"memory");
    MFMA(o6_0, w17_6, h17_0); SCHED();
    MFMA(o6_1, w17_6, h17_1); SCHED();
    const u32x4 w16_1 = LDSR(pW_0, 5120);
    MFMA(o0_0, w15_0, h15_0); SCHED();
    MFMA(o0_1, w15_0, h15_1); SCHED();
    const u32x4 w17_1 = LDSR(pW_0, 7168);
    MFMA(o0_0, w16_0, h16_0); SCHED();
    WIN(pE, 1);
    asm volatile("s_waitcnt vmcnt(4)" : "+v"(fl5)::"memory"); CHECK(fl5, 1u, sFQ, 4);
    MFMA(o0_1, w16_0, h16_1); SCHED();
    const u32x4 w15_2 = LDSR(pW_0, 9216);
    LDSWF(pE_1, 12288, o6_1);
    MFMA(o0_0, w17_0, h17_0); SCHED();
    MFMA(o0_1, w17_0, h17_1); SCHED();
    const u32x4 w16_2 = LDSR(pW_0, 11264);
    MFMA(o1_0, w15_1, h15_0); SCHED();
    MFMA(o1_1, w15_1, h15_1); SCHED();
    WAITV(15); WAITL0(); BAR();
    const u32x4 w17_2 = LDSR(pW_0, 13312);
    const f32x4 xo6 = LDSRF(pE_1, 8192);
    u32x4 gb0, gb1, gb2; GBLOAD();
    MFMA(o1_0, w16_1, h16_0); SCHED();
    MFMA(o1_1, w16_1, h16_1); SCHED();
    const u32x4 w15_3 = LDSR(pW_0, 15360);
    MFMA(o1_0, w17_1, h17_0); SCHED();
    MFMA(o1_1, w17_1, h17_1); SCHED();
    const u32x4 w16_3 = LDSR(pW_0, 17408);
    XADD(6, 0);
    XADD(6, 1);
    MFMA(o2_0, w15_2, h15_0); SCHED();
    XADD(6, 2);
    XADD(6, 3);
    MFMA(o2_1, w15_2, h15_1); SCHED();
    const u32x4 w17_3 = LDSR(pW_0, 19456);
    u32 pp6_0, pp6_1; XPACK(6);
    MFMA(o2_0, w16_2, h16_0); SCHED();
    MFMA(o2_1, w16_2, h16_1); SCHED();
    const u32x4 w15_4 = LDSR(pW_0, 21504);
    LDSWF(pE_1, 20480, o0_1);
    MFMA(o2_0, w17_2, h17_0); SCHED();
    LDSWF(pE_1, 28672, o1_1);
    XST8();
    MFMA(o2_1, w17_2, h17_1); SCHED();
    const u32x4 w16_4 = LDSR(pW_0, 23552);
    MFMA(o3_0, w15_3, h15_0); SCHED();
    MFMA(o3_1, w15_3, h15_1); SCHED();
    WAITV(15); WAITL0(); BAR();
    const u32x4 w17_4 = LDSR(pW_0, 25600);
    const f32x4 xo0 = LDSRF(pE_1, 16384);
    MFMA(o3_0, w16_3, h16_0); SCHED();
    const f32x4 xo1 = LDSRF(pE_1, 24576);
    MFMA(o3_1, w16_3, h16_1); SCHED();
    const u32x4 w15_5 = LDSR(pW_0, 27648);
    MFMA(o3_0, w17_3, h17_0); SCHED();
    MFMA(o3_1, w17_3, h17_1); SCHED();
    const u32x4 w16_5 = LDSR(pW_0, 29696);
    XADD(0, 0);
    XADD(0, 1);
    MFMA(o4_0, w15_4, h15_0); SCHED();
    XADD(0, 2);
    XADD(0, 3);
    MFMA(o4_1, w15_4, h15_1); SCHED();
    const u32x4 w17_5 = LDSR(pW_0, 31744);
    XADD(1, 0);
    XADD(1, 1);
    MFMA(o4_0, w16_4, h16_0); SCHED();
    XADD(1, 2);
    XADD(1, 3);
    MFMA(o4_1, w16_4, h16_1); SCHED();
    const u32x4 w8_7 = LDSR(pW_0, 33792);
    LDSWF(pE_1, 36864, o2_1);
    u32 pp0_0, pp0_1; XPACK(0);
    MFMA(o4_0, w17_4, h17_0); SCHED();
    LDSWF(pE_1, 45056, o3_1);
    MFMA(o4_1, w17_4, h17_1); SCHED();
    const u32x4 w8_8 = LDSR(pW_0, 35840);
    u32 pp1_0, pp1_1; XPACK(1);
    MFMA(o5_0, w15_5, h15_0); SCHED();
    MFMA(o5_1, w15_5, h15_1); SCHED();
    WAITV(6); WAITL0(); BAR();
    const u32x4 w9_7 = LDSR(pW_0, 37888);
    const f32x4 xo2 = LDSRF(pE_1, 32768);
    MFMA(o5_0, w16_5, h16_0); SCHED();
    const f32x4 xo3 = LDSRF(pE_1, 40960);
    XST(0, 0, 1);
    MFMA(o5_1, w16_5, h16_1); SCHED();
    const u32x4 w9_8 = LDSR(pW_0, 39936);
    MFMA(o5_0, w17_5, h17_0); SCHED();
    MFMA(o5_1, w17_5, h17_1); SCHED();
    const u32x4 w10_7 = LDSR(pW_0, 41984);
    XADD(2, 0);
    XADD(2, 1);
    MFMA(o7_0, w8_7, h8_0); SCHED();
    XADD(2, 2);
    XADD(2, 3);
    MFMA(o7_1, w8_7, h8_1); SCHED();
    const u32x4 w10_8 = LDSR(pW_0, 44032);
    XADD(3, 0);
    XADD(3, 1);
    MFMA(o8_0, w8_8, h8_0); SCHED();
    XADD(3, 2);
    XADD(3, 3);
    MFMA(o8_1, w8_8, h8_1); SCHED();
    const u32x4 w11_7 = LDSR(pW_0, 46080);
    LDSWF(pE_1, 53248, o4_1);
    u32 pp2_0, pp2_1; XPACK(2);
    MFMA(o7_0, w9_7, h9_0); SCHED();
    LDSWF(pE_1, 61440, o5_1);
    MFMA(o7_1, w9_7, h9_1); SCHED();
    const u32x4 w11_8 = LDSR(pW_0, 48128);
    u32 pp3_0, pp3_1; XPACK(3);
    MFMA(o8_0, w9_8, h9_0); SCHED();
    MFMA(o8_1, w9_8, h9_1); SCHED();
    WAITV(5); WAITL0(); BAR();
    const u32x4 w12_7 = LDSR(pW_0, 50176);
    const f32x4 xo4 = LDSRF(pE_1, 49152);
    MFMA(o7_0, w10_7, h10_0); SCHED();
    const f32x4 xo5 = LDSRF(pE_1, 57344);
    XST(1, 2, 3);
    MFMA(o7_1, w10_7, h10_1); SCHED();
    const u32x4 w12_8 = LDSR(pW_0, 52224);
    MFMA(o8_0, w10_8, h10_0); SCHED();
    MFMA(o8_1, w10_8, h10_1); SCHED();
    const u32x4 w13_7 = LDSR(pW_0, 54272);
    XADD(4, 0);
    XADD(4, 1);
    MFMA(o7_0, w11_7, h11_0); SCHED();
    XADD(4, 2);
    XADD(4, 3);
    MFMA(o7_1, w11_7, h11_1); SCHED();
    const u32x4 w13_8 = LDSR(pW_0, 56320);
    XADD(5, 0);
    XADD(5, 1);
    MFMA(o8_0, w11_8, h11_0); SCHED();
    XADD(5, 2);
    XADD(5, 3);
    MFMA(o8_1, w11_8, h11_1); SCHED();
    const u32x4 w14_7 = LDSR(pW_0, 58368);
    u32 pp4_0, pp4_1; XPACK(4);
    MFMA(o7_0, w12_7, h12_0); SCHED();
    MFMA(o7_1, w12_7, h12_1); SCHED();
    const u32x4 w14_8 = LDSR(pW_0, 60416);
    u32 pp5_0, pp5_1; XPACK(5);
    MFMA(o8_0, w12_8, h12_0); SCHED();
    MFMA(o8_1, w12_8, h12_1); SCHED();
    const u32x4 w15_7 = LDSR(pW_0, 62464);
    MFMA(o7_0, w13_7, h13_0); SCHED();
    XST(2, 4, 5);
    MFMA(o7_1, w13_7, h13_1); SCHED();
    const u32x4 w15_8 = LDSR(pW_0, 64512);
    MFMA(o8_0, w13_8, h13_0); SCHED();
    MFMA(o8_1, w13_8, h13_1); SCHED();
    const u32x4 w16_7 = LDSR(pW_1, 1024);
    MFMA(o7_0, w14_7, h14_0); SCHED();
    MFMA(o7_1, w14_7, h14_1); SCHED();
    const u32x4 w16_8 = LDSR(pW_1, 3072);
    MFMA(o8_0, w14_8, h14_0); SCHED();
    MFMA(o8_1, w14_8, h14_1); SCHED();
    WAITV(7); BAR();
    const u32x4 w17_7 = LDSR(pW_1, 5120);
    MFMA(o7_0, w15_7, h15_0); SCHED();
    MFMA(o7_1, w15_7, h15_1); SCHED();
    const u32x4 w17_8 = LDSR(pW_1, 7168);
    MFMA(o8_0, w15_8, h15_0); SCHED();
    MFMA(o8_1, w15_8, h15_1); SCHED();
    u32x4 r0, r1, r2, r3, r4, r5; PLOAD();
    MFMA(o7_0, w16_7, h16_0); SCHED();
    MFMA(o7_1, w16_7, h16_1); SCHED();
    MFMA(o8_0, w16_8, h16_0); SCHED();
    MFMA(o8_1, w16_8, h16_1); SCHED();
    MFMA(o7_0, w17_7, h17_0); SCHED();
    MFMA(o7_1, w17_7, h17_1); SCHED();
    MFMA(o8_0, w17_8, h17_0); SCHED();
    MFMA(o8_1, w17_8, h17_1); SCHED();
    TAIL(2, 135168, 143360, 131072, 139264);
  }
}

extern "C" void kernel_launch(void* const* d_in, const int* in_sizes, int n_in, void* d_out, int out_size, void* d_ws,
                              size_t ws_size, hipStream_t stream) {
  const u16* u = (const u16*)d_in[0];
  const u16* lar = (const u16*)d_in[1];
  const u16* aim = (const u16*)d_in[2];
  const u16* cre = (const u16*)d_in[3];
  const u16* cim = (const u16*)d_in[4];
  const u16* ldt = (const u16*)d_in[5];
  const u16* dsk = (const u16*)d_in[6];
  const u16* w1 = (const u16*)d_in[8];
  const u16* w2 = (const u16*)d_in[9];
  const u16* c = (const u16*)d_in[10];
  const u16* wmod = (const u16*)d_in[11];
  char* ws = (char*)d_ws;
  (void)hipFuncSetAttribute((const void*)kc_mlp, hipFuncAttributeMaxDynamicSharedMemorySize, KC_LDS);
  ka_prep<<<KA_LN + KA_MOD + KA_KG + KA_W2, 256, 0, stream>>>(u, lar, aim, cre, cim, ldt, c, wmod, w2, ws);
  kb_conv<<<KB_W1 + KB_W2 + KB_CONV + KB_DUM, 256, 0, stream>>>(dsk, w1, w2, ws);
  kc_mlp<<<256, 512, KC_LDS, stream>>>(ws, (u16*)d_out);
}
```
